```python
import torch

def _softmax(x, axis=-1):
    return torch.softmax(x, dim=axis)
def _gelu(x):
    return torch.nn.functional.gelu(x, approximate="tanh")
def _silu(x):
    return torch.nn.functional.silu(x)
def _sigmoid(x):
    return torch.sigmoid(x)
def _layernorm(x, axis=-1, eps=1e-5):
    m = x.mean(dim=axis, keepdim=True)
    v = x.var(dim=axis, keepdim=True, unbiased=False)
    return (x - m) / torch.sqrt(v + eps)
def _scatter_add(tgt, idx, src, axis=0):
    return tgt.clone().index_add_(axis, idx, src)
def _segment_sum(src, seg, n):
    out = torch.zeros((n,) + src.shape[1:], dtype=src.dtype)
    return out.index_add_(0, seg, src)
def _pad(x, pw):
    flat = tuple(v for pair in reversed(pw) for v in pair)
    return torch.nn.functional.pad(x, flat)

kernel_name = 'standalone__mha_block_cached__decode'

def setup_inputs():

    _w = (lambda a: (lambda t: t.to(torch.bfloat16) if t.is_floating_point() else t)(torch.as_tensor(a)))
    B, Sq, D = 4, 1, 512
    H, Sk, Dh = 32, 4096, 128
    return {
        "x": _w((lambda *s: torch.randn(*s, dtype=torch.bfloat16) * 0.8)(B, Sq, D)),
        "k_cache": _w((lambda *s: torch.randn(*s, dtype=torch.bfloat16) * 0.8)(B, H, Sk, Dh)),
        "v_cache": _w((lambda *s: torch.randn(*s, dtype=torch.bfloat16) * 0.05)(B, H, Sk, Dh)),
        "wq": _w((lambda *s: torch.randn(*s, dtype=torch.bfloat16) * 0.8)(D, H * Dh) * (D ** -0.5)),
        "wo": _w((lambda *s: torch.randn(*s, dtype=torch.bfloat16) * 0.05)(H * Dh, D) * ((H * Dh) ** -0.5)),
    }

def reference(x, k_cache, v_cache, wq, wo):
    B, Sq, D = x.shape
    H, Dh = 32, 128
    q = (lambda x, *d: x.permute(*d))((x @ wq).reshape(B, Sq, H, Dh), 0, 2, 1, 3)
    scores = torch.einsum("bhqd,bhkd->bhqk", q, k_cache) * (Dh ** -0.5)
    p = _softmax(scores, axis=-1)
    o = torch.einsum("bhqk,bhkd->bhqd", p, v_cache)
    return (lambda x, *d: x.permute(*d))(o, 0, 2, 1, 3).reshape(B, Sq, H * Dh) @ wo
```

```cpp
#include <hip/hip_runtime.h>
#include <hip/hip_bf16.h>
#include <cstdint>

typedef unsigned v4u __attribute__((ext_vector_type(4)));
typedef float f32x4 __attribute__((ext_vector_type(4)));
typedef short bf16x8 __attribute__((ext_vector_type(8)));
typedef __bf16 bf16x2 __attribute__((ext_vector_type(2)));

#define KV_AUX 2
#define WIN 16
constexpr int NB = 4, DM = 512, NH = 32, SK = 4096, DH = 128;
constexpr int NWG = 256, NWAVE = 8, NTHR = NWAVE * 64;
constexpr int KEYS_WG = SK / 2;
constexpr int NLD = KEYS_WG / NWAVE / 4;
constexpr int W = WIN;
constexpr float C2 = 0.088388347648318440f * 1.4426950408889634f;
constexpr int RSRC_W3 = 0x00020000;

__device__ __forceinline__ f32x4 mfma(v4u a, v4u b, f32x4 c) {
  return __builtin_amdgcn_mfma_f32_16x16x32_bf16(__builtin_bit_cast(bf16x8, a), __builtin_bit_cast(bf16x8, b), c, 0, 0, 0);
}
__device__ __forceinline__ float dot2(unsigned k, unsigned q, float acc) {
  return __builtin_amdgcn_fdot2_f32_bf16(__builtin_bit_cast(bf16x2, k), __builtin_bit_cast(bf16x2, q), acc, false);
}
template <int CTRL> __device__ __forceinline__ unsigned dppu(unsigned x) { return (unsigned)__builtin_amdgcn_mov_dpp((int)x, CTRL, 0xf, 0xf, true); }
template <int CTRL> __device__ __forceinline__ float dppf(float x) { return __builtin_bit_cast(float, dppu<CTRL>(__builtin_bit_cast(unsigned, x))); }
constexpr int XOR1 = 0xB1, XOR2 = 0x4E, XOR7 = 0x141, XOR8 = 0x128, BCAST = 0x150;
__device__ __forceinline__ unsigned cvtpk(float lo, float hi) {
  bf16x2 r = {(__bf16)lo, (__bf16)hi};
  return __builtin_bit_cast(unsigned, r);
}
__device__ __forceinline__ unsigned split_hilo(float x) {
  const unsigned h = cvtpk(x, 0.f) & 0xffffu;
  const float r = x - __builtin_bit_cast(float, h << 16);
  return cvtpk(0.f, r) | h;
}
__device__ __forceinline__ float xrow_max(float x) {
  auto s = __builtin_amdgcn_permlane16_swap(__float_as_uint(x), __float_as_uint(x), false, false);
  x = fmaxf(__uint_as_float(s[0]), __uint_as_float(s[1]));
  auto t = __builtin_amdgcn_permlane32_swap(__float_as_uint(x), __float_as_uint(x), false, false);
  return fmaxf(__uint_as_float(t[0]), __uint_as_float(t[1]));
}
__device__ __forceinline__ float xrow_sum(float x) {
  auto s = __builtin_amdgcn_permlane16_swap(__float_as_uint(x), __float_as_uint(x), false, false);
  x = __uint_as_float(s[0]) + __uint_as_float(s[1]);
  auto t = __builtin_amdgcn_permlane32_swap(__float_as_uint(x), __float_as_uint(x), false, false);
  return __uint_as_float(t[0]) + __uint_as_float(t[1]);
}
__device__ __forceinline__ float wave_max(float x) {
  x = fmaxf(x, dppf<XOR1>(x)); x = fmaxf(x, dppf<XOR2>(x)); x = fmaxf(x, dppf<XOR7>(x)); x = fmaxf(x, dppf<XOR8>(x));
  return xrow_max(x);
}
__device__ __forceinline__ float wave_sum(float x) {
  x += dppf<XOR1>(x); x += dppf<XOR2>(x); x += dppf<XOR7>(x); x += dppf<XOR8>(x);
  return xrow_sum(x);
}
template <int SRC> __device__ __forceinline__ v4u diagA(unsigned pk, const v4u& sel) {
  const unsigned t = dppu<BCAST + SRC>(pk);
  v4u a;
  a.x = __builtin_amdgcn_perm(t, 0u, sel.x); a.y = __builtin_amdgcn_perm(t, 0u, sel.y);
  a.z = __builtin_amdgcn_perm(t, 0u, sel.z); a.w = __builtin_amdgcn_perm(t, 0u, sel.w);
  return a;
}

constexpr int OB_STR = 132;
constexpr int L_Q = 0;
constexpr int L_ML = L_Q + NWAVE * 128;
constexpr int L_O = L_ML + 32;
constexpr int L_Y = L_O + 16 * OB_STR;
constexpr int L_END = L_Y + 16 * 512;
constexpr int LDS_BYTES = 84 * 1024;
static_assert(L_END * 4 <= LDS_BYTES, "lds");

template <int I> struct IC { static constexpr int v = I; };
template <int N, int I = 0, typename F> __device__ __forceinline__ void sfor(F&& f) {
  if constexpr (I < N) { f(IC<I>{}); sfor<N, I + 1>(f); }
}

__global__ void __launch_bounds__(NTHR, 2) attn_fused(const uint16_t* __restrict__ x, const uint16_t* __restrict__ kc, const uint16_t* __restrict__ vc,
                                                      const uint16_t* __restrict__ wq, const uint16_t* __restrict__ wo, float* __restrict__ ws) {
  extern __shared__ __attribute__((aligned(16))) float lds[];
  const int tid = threadIdx.x, lane = tid & 63, w = __builtin_amdgcn_readfirstlane(tid >> 6), g = lane >> 4, c = lane & 15;
  const int bid = blockIdx.x, h = (bid & 7) * 4 + (bid >> 6), sub = (bid >> 3) & 7, b = sub >> 1, half = sub & 1;
  const size_t kvoff = ((size_t)(b * NH + h) * SK + half * KEYS_WG) * DH + w * 16 * DH;
  const int kvbytes = KEYS_WG * DH * 2 - w * 4096;
  const __amdgpu_buffer_rsrc_t rk = __builtin_amdgcn_make_buffer_rsrc((void*)(kc + kvoff), 0, kvbytes, RSRC_W3);
  const __amdgpu_buffer_rsrc_t rv = __builtin_amdgcn_make_buffer_rsrc((void*)(vc + kvoff), 0, kvbytes, RSRC_W3);
  const __amdgpu_buffer_rsrc_t rq = __builtin_amdgcn_make_buffer_rsrc((void*)(wq + (size_t)(64 * w) * (NH * DH) + h * DH), 0, 63 * NH * DH * 2 + 256, RSRC_W3);
  const __amdgpu_buffer_rsrc_t ro = __builtin_amdgcn_make_buffer_rsrc((void*)(wo + (size_t)(h * DH + 16 * w) * DM), 0, 16 * DM * 2, RSRC_W3);
  const int voff = lane * 16;
  auto ldkv = [&](auto I) -> v4u {
    constexpr int i = decltype(I)::v, j = i % NLD;
    return __builtin_amdgcn_raw_buffer_load_b128(i < NLD ? rk : rv, voff + (j & 3) * 1024, (j >> 2) * (NWAVE * 4096), KV_AUX);
  };
  v4u sel;
  {
    const unsigned src = (c < 8) ? 0x0504u : 0x0706u;
    const unsigned full = (c & 1) ? (src << 16 | 0x0c0cu) : (0x0c0c0000u | src);
    const int d = (c & 7) >> 1;
    sel.x = d == 0 ? full : 0x0c0c0c0cu; sel.y = d == 1 ? full : 0x0c0c0c0cu; sel.z = d == 2 ? full : 0x0c0c0c0cu; sel.w = d == 3 ? full : 0x0c0c0c0cu;
  }
  const unsigned xv = x[b * DM + 64 * w + 4 * c + g];
  v4u a[W], wr[16];
  constexpr int PRE = 8;
  sfor<PRE>([&](auto I) { a[I.v] = ldkv(I); });
  __builtin_amdgcn_sched_barrier(0);
  sfor<16>([&](auto I) { wr[I.v] = __builtin_amdgcn_raw_buffer_load_b128(rq, g * (NH * DH * 2) + c * 16, I.v * 4 * (NH * DH * 2), 0); });
  __builtin_amdgcn_sched_barrier(0);
  sfor<W - PRE>([&](auto I) { a[PRE + I.v] = ldkv(IC<PRE + I.v>{}); });
  __builtin_amdgcn_sched_barrier(0);
  {
    f32x4 qa = {0.f, 0.f, 0.f, 0.f};
    sfor<16>([&](auto I) { qa = mfma(diagA<I.v>(xv, sel), wr[I.v], qa); });
    if (g < 2) *reinterpret_cast<f32x4*>(lds + L_Q + w * 128 + 8 * c + 4 * g) = qa;
  }
  sfor<16>([&](auto I) { wr[I.v] = __builtin_amdgcn_raw_buffer_load_b128(ro, g * (DM * 2) + c * 16 + (I.v & 3) * 256, (I.v >> 2) * 4 * (DM * 2), 0); });
  __syncthreads();
  v4u qp;
  {
    f32x4 s0 = {0.f, 0.f, 0.f, 0.f}, s1 = {0.f, 0.f, 0.f, 0.f};
#pragma unroll
    for (int u = 0; u < NWAVE; ++u) {
      s0 += *reinterpret_cast<const f32x4*>(lds + L_Q + u * 128 + 8 * c);
      s1 += *reinterpret_cast<const f32x4*>(lds + L_Q + u * 128 + 8 * c + 4);
    }
    qp.x = cvtpk(s0.x, s0.y); qp.y = cvtpk(s0.z, s0.w); qp.z = cvtpk(s1.x, s1.y); qp.w = cvtpk(s1.z, s1.w);
  }
  __builtin_amdgcn_sched_barrier(0);
  float sc[4], xs[16];
  sfor<NLD>([&](auto I) {
    constexpr int i = I.v;
    const v4u cur = a[i % W];
    a[i % W] = ldkv(IC<i + W>{});
    float d = dot2(cur.x, qp.x, 0.f); d = dot2(cur.y, qp.y, d); d = dot2(cur.z, qp.z, d); d = dot2(cur.w, qp.w, d);
    xs[i & 15] = d;
    if constexpr ((i & 15) == 15) {
#pragma unroll
      for (int u = 0; u < 8; ++u) { const float t1 = xs[u] + dppf<XOR8>(xs[u]), t2 = xs[u + 8] + dppf<XOR8>(xs[u + 8]); xs[u] = (c & 8) ? t2 : t1; }
#pragma unroll
      for (int u = 0; u < 4; ++u) { const float t1 = xs[u] + dppf<XOR7>(xs[u]), t2 = xs[u + 4] + dppf<XOR7>(xs[u + 4]); xs[u] = (c & 4) ? t2 : t1; }
#pragma unroll
      for (int u = 0; u < 2; ++u) { const float t1 = xs[u] + dppf<XOR2>(xs[u]), t2 = xs[u + 2] + dppf<XOR2>(xs[u + 2]); xs[u] = (c & 2) ? t2 : t1; }
      { const float t1 = xs[0] + dppf<XOR1>(xs[0]), t2 = xs[1] + dppf<XOR1>(xs[1]); sc[i >> 4] = (c & 1) ? t2 : t1; }
    }
    __builtin_amdgcn_sched_barrier(0);
  });
  {
    const float mw = wave_max(fmaxf(fmaxf(sc[0], sc[1]), fmaxf(sc[2], sc[3])));
    if (lane == 0) lds[L_ML + w] = mw;
  }
  __syncthreads();
  float M;
  {
    const f32x4 m0 = *reinterpret_cast<const f32x4*>(lds + L_ML), m1 = *reinterpret_cast<const f32x4*>(lds + L_ML + 4);
    M = fmaxf(fmaxf(fmaxf(m0.x, m0.y), fmaxf(m0.z, m0.w)), fmaxf(fmaxf(m1.x, m1.y), fmaxf(m1.z, m1.w)));
  }
  unsigned pk[4];
  {
    float l = 0.f;
#pragma unroll
    for (int r = 0; r < 4; ++r) { const float p = __builtin_amdgcn_exp2f((sc[r] - M) * C2); l += p; pk[r] = split_hilo(p); }
    l = wave_sum(l);
    if (lane == 0) lds[L_ML + 8 + w] = l;
  }
  __builtin_amdgcn_sched_barrier(0);
  f32x4 oa = {0.f, 0.f, 0.f, 0.f};
  sfor<NLD>([&](auto I) {
    constexpr int i = I.v, s = NLD + i;
    const v4u cur = a[s % W];
    if constexpr (s + W < 2 * NLD) a[s % W] = ldkv(IC<s + W>{});
    oa = mfma(diagA<i & 15>(pk[i >> 4], sel), cur, oa);
    __builtin_amdgcn_sched_barrier(0);
  });
  *reinterpret_cast<f32x4*>(lds + L_O + (2 * w + (g >> 1)) * OB_STR + 8 * c + 4 * (g & 1)) = oa;
  __syncthreads();
  unsigned op;
  {
    const float* src = lds + L_O + (4 * (c & 3)) * OB_STR + 16 * w + (c & 12) + g;
    float o = (src[0] + src[OB_STR]) + (src[2 * OB_STR] + src[3 * OB_STR]);
    o += dppf<XOR1>(o); o += dppf<XOR2>(o);
    op = split_hilo(o);
  }
  sfor<4>([&](auto NBK) {
    f32x4 y = {0.f, 0.f, 0.f, 0.f};
    sfor<4>([&](auto I) { y = mfma(diagA<4 * I.v>(op, sel), wr[I.v * 4 + NBK.v], y); });
    *reinterpret_cast<f32x4*>(lds + L_Y + (2 * w + (g >> 1)) * 512 + 128 * NBK.v + 8 * c + 4 * (g & 1)) = y;
  });
  __syncthreads();
  {
    float y = 0.f;
#pragma unroll
    for (int u = 0; u < 16; ++u) y += lds[L_Y + u * 512 + tid];
    const int slot = b * 64 + h * 2 + half;
    ws[slot * 512 + tid] = y;
    if (tid == 0) {
      float L = 0.f;
#pragma unroll
      for (int u = 0; u < NWAVE; ++u) L += lds[L_ML + 8 + u];
      ws[NWG * 512 + slot * 2] = M; ws[NWG * 512 + slot * 2 + 1] = L;
    }
  }
}

__global__ void __launch_bounds__(512) reduce_out(const float* __restrict__ ws, __hip_bfloat16* __restrict__ out) {
  __shared__ float part[8][64];
  const int lane = threadIdx.x & 63, w = threadIdx.x >> 6, b = blockIdx.y, n = blockIdx.x * 64 + lane;
  float acc = 0.f;
#pragma unroll
  for (int u = 0; u < 4; ++u) {
    const int slot = b * 64 + (w * 4 + u) * 2;
    const f32x4 ml = *reinterpret_cast<const f32x4*>(ws + NWG * 512 + slot * 2);
    const float y0 = ws[slot * 512 + n], y1 = ws[(slot + 1) * 512 + n];
    const float M = fmaxf(ml.x, ml.z), w0 = __builtin_amdgcn_exp2f((ml.x - M) * C2), w1 = __builtin_amdgcn_exp2f((ml.z - M) * C2);
    acc += (w0 * y0 + w1 * y1) / (w0 * ml.y + w1 * ml.w);
  }
  part[w][lane] = acc;
  __syncthreads();
  if (w == 0) {
    float s = 0.f;
#pragma unroll
    for (int u = 0; u < 8; ++u) s += part[u][lane];
    out[b * DM + n] = __float2bfloat16(s);
  }
}

extern "C" void kernel_launch(void* const* d_in, const int* in_sizes, int n_in, void* d_out, int out_size, void* d_ws, size_t ws_size, hipStream_t stream) {
  (void)in_sizes; (void)n_in; (void)out_size; (void)ws_size;
  hipFuncSetAttribute((const void*)attn_fused, hipFuncAttributeMaxDynamicSharedMemorySize, LDS_BYTES);
  attn_fused<<<NWG, NTHR, LDS_BYTES, stream>>>((const uint16_t*)d_in[0], (const uint16_t*)d_in[1], (const uint16_t*)d_in[2], (const uint16_t*)d_in[3], (const uint16_t*)d_in[4], (float*)d_ws);
  reduce_out<<<dim3(8, 4), 512, 0, stream>>>((const float*)d_ws, (__hip_bfloat16*)d_out);
}
```

```cpp
#include <hip/hip_runtime.h>
#include <hip/hip_bf16.h>
#include <cstdint>

typedef unsigned v4u __attribute__((ext_vector_type(4)));
typedef float f32x4 __attribute__((ext_vector_type(4)));
typedef short bf16x8 __attribute__((ext_vector_type(8)));
typedef __bf16 bf16x2 __attribute__((ext_vector_type(2)));

#define K_AUX 2
#define V_AUX 2
#define WIN 24
constexpr int NB = 4, DM = 512, NH = 32, SK = 4096, DH = 128;
constexpr int NWG = 256, NWAVE = 8, NTHR = NWAVE * 64;
constexpr int KEYS_WG = SK / 2;
constexpr int NLD = KEYS_WG / NWAVE / 4;
constexpr int W = WIN;
constexpr float C2 = 0.088388347648318440f * 1.4426950408889634f;
constexpr int RSRC_W3 = 0x00020000;

__device__ __forceinline__ f32x4 mfma(v4u a, v4u b, f32x4 c) {
  return __builtin_amdgcn_mfma_f32_16x16x32_bf16(__builtin_bit_cast(bf16x8, a), __builtin_bit_cast(bf16x8, b), c, 0, 0, 0);
}
__device__ __forceinline__ float dot2(unsigned k, unsigned q, float acc) {
  return __builtin_amdgcn_fdot2_f32_bf16(__builtin_bit_cast(bf16x2, k), __builtin_bit_cast(bf16x2, q), acc, false);
}
template <int CTRL> __device__ __forceinline__ unsigned dppu(unsigned x) { return (unsigned)__builtin_amdgcn_mov_dpp((int)x, CTRL, 0xf, 0xf, true); }
template <int CTRL> __device__ __forceinline__ float dppf(float x) { return __builtin_bit_cast(float, dppu<CTRL>(__builtin_bit_cast(unsigned, x))); }
constexpr int XOR1 = 0xB1, XOR2 = 0x4E, XOR7 = 0x141, XOR8 = 0x128, BCAST = 0x150;
__device__ __forceinline__ unsigned cvtpk(float lo, float hi) {
  bf16x2 r = {(__bf16)lo, (__bf16)hi};
  return __builtin_bit_cast(unsigned, r);
}
__device__ __forceinline__ unsigned split_hilo(float x) {
  const unsigned h = cvtpk(x, 0.f) & 0xffffu;
  const float r = x - __builtin_bit_cast(float, h << 16);
  return cvtpk(0.f, r) | h;
}
__device__ __forceinline__ float xrow_max(float x) {
  auto s = __builtin_amdgcn_permlane16_swap(__float_as_uint(x), __float_as_uint(x), false, false);
  x = fmaxf(__uint_as_float(s[0]), __uint_as_float(s[1]));
  auto t = __builtin_amdgcn_permlane32_swap(__float_as_uint(x), __float_as_uint(x), false, false);
  return fmaxf(__uint_as_float(t[0]), __uint_as_float(t[1]));
}
__device__ __forceinline__ float xrow_sum(float x) {
  auto s = __builtin_amdgcn_permlane16_swap(__float_as_uint(x), __float_as_uint(x), false, false);
  x = __uint_as_float(s[0]) + __uint_as_float(s[1]);
  auto t = __builtin_amdgcn_permlane32_swap(__float_as_uint(x), __float_as_uint(x), false, false);
  return __uint_as_float(t[0]) + __uint_as_float(t[1]);
}
__device__ __forceinline__ float wave_max(float x) {
  x = fmaxf(x, dppf<XOR1>(x)); x = fmaxf(x, dppf<XOR2>(x)); x = fmaxf(x, dppf<XOR7>(x)); x = fmaxf(x, dppf<XOR8>(x));
  return xrow_max(x);
}
__device__ __forceinline__ float wave_sum(float x) {
  x += dppf<XOR1>(x); x += dppf<XOR2>(x); x += dppf<XOR7>(x); x += dppf<XOR8>(x);
  return xrow_sum(x);
}
template <int SRC> __device__ __forceinline__ v4u diagA(unsigned pk, const v4u& sel) {
  const unsigned t = dppu<BCAST + SRC>(pk);
  v4u a;
  a.x = __builtin_amdgcn_perm(t, 0u, sel.x); a.y = __builtin_amdgcn_perm(t, 0u, sel.y);
  a.z = __builtin_amdgcn_perm(t, 0u, sel.z); a.w = __builtin_amdgcn_perm(t, 0u, sel.w);
  return a;
}

constexpr int OB_STR = 132;
constexpr int L_Q = 0;
constexpr int L_ML = L_Q + NWAVE * 128;
constexpr int L_O = L_ML + 32;
constexpr int L_Y = L_O + 16 * OB_STR;
constexpr int L_END = L_Y + 16 * 512;
constexpr int LDS_BYTES = 84 * 1024;
static_assert(L_END * 4 <= LDS_BYTES, "lds");

#define STAMP(k) do {} while (0)
template <int I> struct IC { static constexpr int v = I; };
template <int N, int I = 0, typename F> __device__ __forceinline__ void sfor(F&& f) {
  if constexpr (I < N) { f(IC<I>{}); sfor<N, I + 1>(f); }
}

__global__ void __launch_bounds__(NTHR, 2) attn_fused(const uint16_t* __restrict__ x, const uint16_t* __restrict__ kc, const uint16_t* __restrict__ vc,
                                                      const uint16_t* __restrict__ wq, const uint16_t* __restrict__ wo, float* __restrict__ ws) {
  extern __shared__ __attribute__((aligned(16))) float lds[];
  STAMP(0);
  const int tid = threadIdx.x, lane = tid & 63, w = __builtin_amdgcn_readfirstlane(tid >> 6), g = lane >> 4, c = lane & 15;
  const int bid = blockIdx.x, h = (bid & 7) * 4 + (bid >> 6), sub = (bid >> 3) & 7, b = sub >> 1, half = sub & 1;
  const size_t kvoff = ((size_t)(b * NH + h) * SK + half * KEYS_WG) * DH + w * 16 * DH;
  const int kvbytes = KEYS_WG * DH * 2 - w * 4096;
  const __amdgpu_buffer_rsrc_t rk = __builtin_amdgcn_make_buffer_rsrc((void*)(kc + kvoff), 0, kvbytes, RSRC_W3);
  const __amdgpu_buffer_rsrc_t rv = __builtin_amdgcn_make_buffer_rsrc((void*)(vc + kvoff), 0, kvbytes, RSRC_W3);
  const __amdgpu_buffer_rsrc_t rq = __builtin_amdgcn_make_buffer_rsrc((void*)(wq + (size_t)(64 * w) * (NH * DH) + h * DH), 0, 63 * NH * DH * 2 + 256, RSRC_W3);
  const __amdgpu_buffer_rsrc_t ro = __builtin_amdgcn_make_buffer_rsrc((void*)(wo + (size_t)(h * DH + 16 * w) * DM), 0, 16 * DM * 2, RSRC_W3);
  const int voff = lane * 16;
  auto ldkv = [&](auto I) -> v4u {
    constexpr int i = decltype(I)::v, j = i % NLD;
    return __builtin_amdgcn_raw_buffer_load_b128(i < NLD ? rk : rv, voff + (j & 3) * 1024, (j >> 2) * (NWAVE * 4096), i < NLD ? K_AUX : V_AUX);
  };
  v4u sel;
  {
    const unsigned src = (c < 8) ? 0x0504u : 0x0706u;
    const unsigned full = (c & 1) ? (src << 16 | 0x0c0cu) : (0x0c0c0000u | src);
    const int d = (c & 7) >> 1;
    sel.x = d == 0 ? full : 0x0c0c0c0cu; sel.y = d == 1 ? full : 0x0c0c0c0cu; sel.z = d == 2 ? full : 0x0c0c0c0cu; sel.w = d == 3 ? full : 0x0c0c0c0cu;
  }
  const unsigned xv = x[b * DM + 64 * w + 4 * c + g];
  v4u a[W], wr[16];
  constexpr int PRE = 8;
  sfor<PRE>([&](auto I) { a[I.v] = ldkv(I); });
  __builtin_amdgcn_sched_barrier(0);
  sfor<16>([&](auto I) { wr[I.v] = __builtin_amdgcn_raw_buffer_load_b128(rq, g * (NH * DH * 2) + c * 16, I.v * 4 * (NH * DH * 2), 0); });
  __builtin_amdgcn_sched_barrier(0);
  sfor<W - PRE>([&](auto I) { a[PRE + I.v] = ldkv(IC<PRE + I.v>{}); });
  __builtin_amdgcn_sched_barrier(0);
  {
    f32x4 qa = {0.f, 0.f, 0.f, 0.f};
    sfor<16>([&](auto I) { qa = mfma(diagA<I.v>(xv, sel), wr[I.v], qa); });
    if (g < 2) *reinterpret_cast<f32x4*>(lds + L_Q + w * 128 + 8 * c + 4 * g) = qa;
  }
  STAMP(1);
  sfor<16>([&](auto I) { wr[I.v] = __builtin_amdgcn_raw_buffer_load_b128(ro, g * (DM * 2) + c * 16 + (I.v & 3) * 256, (I.v >> 2) * 4 * (DM * 2), 0); });
  __syncthreads();
  v4u qp;
  {
    f32x4 s0 = {0.f, 0.f, 0.f, 0.f}, s1 = {0.f, 0.f, 0.f, 0.f};
#pragma unroll
    for (int u = 0; u < NWAVE; ++u) {
      s0 += *reinterpret_cast<const f32x4*>(lds + L_Q + u * 128 + 8 * c);
      s1 += *reinterpret_cast<const f32x4*>(lds + L_Q + u * 128 + 8 * c + 4);
    }
    qp.x = cvtpk(s0.x, s0.y); qp.y = cvtpk(s0.z, s0.w); qp.z = cvtpk(s1.x, s1.y); qp.w = cvtpk(s1.z, s1.w);
  }
  __builtin_amdgcn_sched_barrier(0);
  STAMP(2);
  float sc[4], xs[16];
  sfor<NLD>([&](auto I) {
    constexpr int i = I.v;
    const v4u cur = a[i % W];
    a[i % W] = ldkv(IC<i + W>{});
    float d = dot2(cur.x, qp.x, 0.f); d = dot2(cur.y, qp.y, d); d = dot2(cur.z, qp.z, d); d = dot2(cur.w, qp.w, d);
    xs[i & 15] = d;
    if constexpr ((i & 15) == 15) {
#pragma unroll
      for (int u = 0; u < 8; ++u) { const float t1 = xs[u] + dppf<XOR8>(xs[u]), t2 = xs[u + 8] + dppf<XOR8>(xs[u + 8]); xs[u] = (c & 8) ? t2 : t1; }
#pragma unroll
      for (int u = 0; u < 4; ++u) { const float t1 = xs[u] + dppf<XOR7>(xs[u]), t2 = xs[u + 4] + dppf<XOR7>(xs[u + 4]); xs[u] = (c & 4) ? t2 : t1; }
#pragma unroll
      for (int u = 0; u < 2; ++u) { const float t1 = xs[u] + dppf<XOR2>(xs[u]), t2 = xs[u + 2] + dppf<XOR2>(xs[u + 2]); xs[u] = (c & 2) ? t2 : t1; }
      { const float t1 = xs[0] + dppf<XOR1>(xs[0]), t2 = xs[1] + dppf<XOR1>(xs[1]); sc[i >> 4] = (c & 1) ? t2 : t1; }
    }
    __builtin_amdgcn_sched_barrier(0);
  });
  STAMP(3);
  {
    const float mw = wave_max(fmaxf(fmaxf(sc[0], sc[1]), fmaxf(sc[2], sc[3])));
    if (lane == 0) lds[L_ML + w] = mw;
  }
  __syncthreads();
  float M;
  {
    const f32x4 m0 = *reinterpret_cast<const f32x4*>(lds + L_ML), m1 = *reinterpret_cast<const f32x4*>(lds + L_ML + 4);
    M = fmaxf(fmaxf(fmaxf(m0.x, m0.y), fmaxf(m0.z, m0.w)), fmaxf(fmaxf(m1.x, m1.y), fmaxf(m1.z, m1.w)));
  }
  unsigned pk[4];
  {
    float l = 0.f;
#pragma unroll
    for (int r = 0; r < 4; ++r) { const float p = __builtin_amdgcn_exp2f((sc[r] - M) * C2); l += p; pk[r] = split_hilo(p); }
    l = wave_sum(l);
    if (lane == 0) lds[L_ML + 8 + w] = l;
  }
  __builtin_amdgcn_sched_barrier(0);
  STAMP(4);
  f32x4 oa = {0.f, 0.f, 0.f, 0.f};
  sfor<NLD>([&](auto I) {
    constexpr int i = I.v, s = NLD + i;
    const v4u cur = a[s % W];
    if constexpr (s + W < 2 * NLD) a[s % W] = ldkv(IC<s + W>{});
    oa = mfma(diagA<i & 15>(pk[i >> 4], sel), cur, oa);
    __builtin_amdgcn_sched_barrier(0);
  });
  STAMP(5);
  *reinterpret_cast<f32x4*>(lds + L_O + (2 * w + (g >> 1)) * OB_STR + 8 * c + 4 * (g & 1)) = oa;
  __syncthreads();
  unsigned op;
  {
    const float* src = lds + L_O + (4 * (c & 3)) * OB_STR + 16 * w + (c & 12) + g;
    float o = (src[0] + src[OB_STR]) + (src[2 * OB_STR] + src[3 * OB_STR]);
    o += dppf<XOR1>(o); o += dppf<XOR2>(o);
    op = split_hilo(o);
  }
  sfor<4>([&](auto NBK) {
    f32x4 y = {0.f, 0.f, 0.f, 0.f};
    sfor<4>([&](auto I) { y = mfma(diagA<4 * I.v>(op, sel), wr[I.v * 4 + NBK.v], y); });
    *reinterpret_cast<f32x4*>(lds + L_Y + (2 * w + (g >> 1)) * 512 + 128 * NBK.v + 8 * c + 4 * (g & 1)) = y;
  });
  __syncthreads();
  {
    float y = 0.f;
#pragma unroll
    for (int u = 0; u < 16; ++u) y += lds[L_Y + u * 512 + tid];
    const int slot = b * 64 + h * 2 + half;
    ws[slot * 512 + tid] = y;
    if (tid == 0) {
      float L = 0.f;
#pragma unroll
      for (int u = 0; u < NWAVE; ++u) L += lds[L_ML + 8 + u];
      ws[NWG * 512 + slot * 2] = M; ws[NWG * 512 + slot * 2 + 1] = L;
    }
  }
  STAMP(6);
}

__global__ void __launch_bounds__(512) reduce_out(const float* __restrict__ ws, __hip_bfloat16* __restrict__ out) {
  __shared__ float part[8][64];
  const int lane = threadIdx.x & 63, w = threadIdx.x >> 6, b = blockIdx.y, n = blockIdx.x * 64 + lane;
  float acc = 0.f;
#pragma unroll
  for (int u = 0; u < 4; ++u) {
    const int slot = b * 64 + (w * 4 + u) * 2;
    const f32x4 ml = *reinterpret_cast<const f32x4*>(ws + NWG * 512 + slot * 2);
    const float y0 = ws[slot * 512 + n], y1 = ws[(slot + 1) * 512 + n];
    const float M = fmaxf(ml.x, ml.z), w0 = __builtin_amdgcn_exp2f((ml.x - M) * C2), w1 = __builtin_amdgcn_exp2f((ml.z - M) * C2);
    acc += (w0 * y0 + w1 * y1) / (w0 * ml.y + w1 * ml.w);
  }
  part[w][lane] = acc;
  __syncthreads();
  if (w == 0) {
    float s = 0.f;
#pragma unroll
    for (int u = 0; u < 8; ++u) s += part[u][lane];
    out[b * DM + n] = __float2bfloat16(s);
  }
}

extern "C" void kernel_launch(void* const* d_in, const int* in_sizes, int n_in, void* d_out, int out_size, void* d_ws, size_t ws_size, hipStream_t stream) {
  (void)in_sizes; (void)n_in; (void)out_size; (void)ws_size;
  hipFuncSetAttribute((const void*)attn_fused, hipFuncAttributeMaxDynamicSharedMemorySize, LDS_BYTES);
  attn_fused<<<NWG, NTHR, LDS_BYTES, stream>>>((const uint16_t*)d_in[0], (const uint16_t*)d_in[1], (const uint16_t*)d_in[2], (const uint16_t*)d_in[3], (const uint16_t*)d_in[4], (float*)d_ws);
  reduce_out<<<dim3(8, 4), 512, 0, stream>>>((const float*)d_ws, (__hip_bfloat16*)d_out);
}
```

```cpp
#include <hip/hip_runtime.h>
#include <hip/hip_bf16.h>
#include <cstdint>

typedef unsigned v4u __attribute__((ext_vector_type(4)));
typedef float f32x4 __attribute__((ext_vector_type(4)));
typedef float f32x2 __attribute__((ext_vector_type(2)));
typedef short bf16x8 __attribute__((ext_vector_type(8)));
typedef __bf16 bf16x2 __attribute__((ext_vector_type(2)));
typedef __attribute__((address_space(1))) unsigned long long gu64;

#define WIN 16
#define PREK 0
#define POLL_SLEEP 4
constexpr int DM = 512, NH = 32, SK = 4096, DH = 128;
constexpr int NWG = 256, NWAVE = 8, NTHR = NWAVE * 64;
constexpr int KEYS_WG = SK / 2;
constexpr int NLD = KEYS_WG / NWAVE / 4;
constexpr int W = WIN, NIT = NLD / W, GPI = W / 16;
constexpr int TSTR = NWAVE * 4096;
constexpr float C2 = 0.088388347648318440f * 1.4426950408889634f;
constexpr int RSRC_W3 = 0x00020000, AUX_NT = 2, AUX_SC1 = 16;
constexpr unsigned SPIN_CAP = 1u << 18;
constexpr int WS_EPOCH = 0, WS_ML = 4096, WS_Y = 8192, WS_END = WS_Y + NWG * 4096;

__device__ __forceinline__ f32x4 mfma(v4u a, v4u b, f32x4 c) {
  return __builtin_amdgcn_mfma_f32_16x16x32_bf16(__builtin_bit_cast(bf16x8, a), __builtin_bit_cast(bf16x8, b), c, 0, 0, 0);
}
__device__ __forceinline__ float dot2(unsigned k, unsigned q, float acc) {
  return __builtin_amdgcn_fdot2_f32_bf16(__builtin_bit_cast(bf16x2, k), __builtin_bit_cast(bf16x2, q), acc, false);
}
template <int CTRL> __device__ __forceinline__ unsigned dppu(unsigned x) { return (unsigned)__builtin_amdgcn_mov_dpp((int)x, CTRL, 0xf, 0xf, true); }
template <int CTRL> __device__ __forceinline__ float dppf(float x) { return __builtin_bit_cast(float, dppu<CTRL>(__builtin_bit_cast(unsigned, x))); }
constexpr int XOR1 = 0xB1, XOR2 = 0x4E, XOR7 = 0x141, XOR8 = 0x128, ROR4 = 0x124, BCAST = 0x150;
__device__ __forceinline__ unsigned cvtpk(float lo, float hi) {
  bf16x2 r = {(__bf16)lo, (__bf16)hi};
  return __builtin_bit_cast(unsigned, r);
}
__device__ __forceinline__ unsigned split_hilo(float x) {
  const unsigned h = cvtpk(x, 0.f);
  return cvtpk(__builtin_bit_cast(float, h << 16), x - __builtin_bit_cast(float, h << 16));
}
__device__ __forceinline__ float wave_max(float x) {
  x = fmaxf(x, dppf<XOR1>(x)); x = fmaxf(x, dppf<XOR2>(x)); x = fmaxf(x, dppf<XOR7>(x)); x = fmaxf(x, dppf<XOR8>(x));
  auto s = __builtin_amdgcn_permlane16_swap(__float_as_uint(x), __float_as_uint(x), false, false);
  x = fmaxf(__uint_as_float(s[0]), __uint_as_float(s[1]));
  auto t = __builtin_amdgcn_permlane32_swap(__float_as_uint(x), __float_as_uint(x), false, false);
  return fmaxf(__uint_as_float(t[0]), __uint_as_float(t[1]));
}
__device__ __forceinline__ float wave_sum(float x) {
  x += dppf<XOR1>(x); x += dppf<XOR2>(x); x += dppf<XOR7>(x); x += dppf<XOR8>(x);
  auto s = __builtin_amdgcn_permlane16_swap(__float_as_uint(x), __float_as_uint(x), false, false);
  x = __uint_as_float(s[0]) + __uint_as_float(s[1]);
  auto t = __builtin_amdgcn_permlane32_swap(__float_as_uint(x), __float_as_uint(x), false, false);
  return __uint_as_float(t[0]) + __uint_as_float(t[1]);
}
template <int SRC> __device__ __forceinline__ v4u diagA(unsigned pk, const v4u& sel) {
  const unsigned t = dppu<BCAST + SRC>(pk);
  v4u a;
  a.x = __builtin_amdgcn_perm(t, 0u, sel.x); a.y = __builtin_amdgcn_perm(t, 0u, sel.y);
  a.z = __builtin_amdgcn_perm(t, 0u, sel.z); a.w = __builtin_amdgcn_perm(t, 0u, sel.w);
  return a;
}

constexpr int OB_STR = 132;
constexpr int L_Q = 0;
constexpr int L_ML = L_Q + NWAVE * 128;
constexpr int L_O = L_ML + 32;
constexpr int L_Y = L_O + 16 * OB_STR;
constexpr int L_R = L_Y + NWAVE * 512;
constexpr int L_END = L_R + 128;
constexpr int LDS_BYTES = 84 * 1024;
static_assert(L_END * 4 <= LDS_BYTES, "lds");

#define STAMP(k) do {} while (0)
#define SB() __builtin_amdgcn_sched_barrier(0)
#define PIN(x) asm volatile("" : "+v"(x) :: "memory")
template <int I> struct IC { static constexpr int v = I; };
template <int N, int I = 0, typename F> __device__ __forceinline__ void sfor(F&& f) {
  if constexpr (I < N) { f(IC<I>{}); sfor<N, I + 1>(f); }
}
__device__ __forceinline__ v4u ldnt(__amdgpu_buffer_rsrc_t r, int voff, int i, int soff) {
  return __builtin_amdgcn_raw_buffer_load_b128(r, voff + (i & 3) * 1024, soff + (i >> 2) * TSTR, AUX_NT);
}

__global__ void __launch_bounds__(NTHR, 2) attn_fused(const uint16_t* __restrict__ x, const uint16_t* __restrict__ kc, const uint16_t* __restrict__ vc,
                                                      const uint16_t* __restrict__ wq, const uint16_t* __restrict__ wo, unsigned char* ws, uint16_t* out) {
  extern __shared__ __attribute__((aligned(16))) float lds[];
  STAMP(0);
  const int tid = threadIdx.x, lane = tid & 63, w = __builtin_amdgcn_readfirstlane(tid >> 6), g = lane >> 4, c = lane & 15;
  const int bid = blockIdx.x, h = (bid & 7) * 4 + (bid >> 6), sub = (bid >> 3) & 7, b = sub >> 1, half = sub & 1;
  const size_t kvoff = ((size_t)(b * NH + h) * SK + half * KEYS_WG) * DH + w * 16 * DH;
  const int kvbytes = KEYS_WG * DH * 2 - w * 4096;
  const __amdgpu_buffer_rsrc_t rk = __builtin_amdgcn_make_buffer_rsrc((void*)(kc + kvoff), 0, kvbytes, RSRC_W3);
  const __amdgpu_buffer_rsrc_t rv = __builtin_amdgcn_make_buffer_rsrc((void*)(vc + kvoff), 0, kvbytes, RSRC_W3);
  const __amdgpu_buffer_rsrc_t rq = __builtin_amdgcn_make_buffer_rsrc((void*)(wq + (size_t)(64 * w) * (NH * DH) + h * DH), 0, 63 * NH * DH * 2 + 256, RSRC_W3);
  const __amdgpu_buffer_rsrc_t ro = __builtin_amdgcn_make_buffer_rsrc((void*)(wo + (size_t)(h * DH + 16 * w) * DM), 0, 16 * DM * 2, RSRC_W3);
  const int voff = lane * 16;
  __builtin_amdgcn_s_setprio(3);
  const unsigned xv = x[b * DM + 64 * w + 4 * c + g];
  const unsigned long long epoch = __hip_atomic_load((gu64*)(ws + WS_EPOCH + b * 256), __ATOMIC_RELAXED, __HIP_MEMORY_SCOPE_AGENT);
  v4u a[W], wr[16];
  sfor<PREK>([&](auto I) { a[I.v] = ldnt(rk, voff, I.v, 0); });
  SB();
  sfor<16>([&](auto I) { wr[I.v] = __builtin_amdgcn_raw_buffer_load_b128(rq, g * (NH * DH * 2) + c * 16, I.v * 4 * (NH * DH * 2), 0); SB(); });
  __builtin_amdgcn_s_setprio(0);
  sfor<W - PREK>([&](auto I) { a[PREK + I.v] = ldnt(rk, voff, PREK + I.v, 0); SB(); });
  v4u sel;
  {
    const unsigned src = (c < 8) ? 0x0504u : 0x0706u;
    const unsigned full = (c & 1) ? (src << 16 | 0x0c0cu) : (0x0c0c0000u | src);
    const int d = (c & 7) >> 1;
    sel.x = d == 0 ? full : 0x0c0c0c0cu; sel.y = d == 1 ? full : 0x0c0c0c0cu; sel.z = d == 2 ? full : 0x0c0c0c0cu; sel.w = d == 3 ? full : 0x0c0c0c0cu;
  }
  {
    f32x4 qa = {0.f, 0.f, 0.f, 0.f};
    sfor<16>([&](auto I) { qa = mfma(diagA<I.v>(xv, sel), wr[I.v], qa); PIN(qa); });
    if (g < 2) *reinterpret_cast<f32x4*>(lds + L_Q + w * 128 + 8 * c + 4 * g) = qa;
  }
  const unsigned tag32 = __builtin_amdgcn_readfirstlane((unsigned)(epoch + 1)), tag8 = __builtin_amdgcn_readfirstlane((unsigned)((epoch + 1) >> 32));
  SB();
  STAMP(1);
  sfor<16>([&](auto I) { wr[I.v] = __builtin_amdgcn_raw_buffer_load_b128(ro, g * (DM * 2) + c * 16 + (I.v & 3) * 256, (I.v >> 2) * 4 * (DM * 2), 0); SB(); });
  __syncthreads();
  v4u qp;
  {
    f32x4 s0 = *reinterpret_cast<const f32x4*>(lds + L_Q + 8 * c), s1 = *reinterpret_cast<const f32x4*>(lds + L_Q + 8 * c + 4);
#pragma unroll
    for (int u = 1; u < NWAVE; ++u) {
      s0 += *reinterpret_cast<const f32x4*>(lds + L_Q + u * 128 + 8 * c);
      s1 += *reinterpret_cast<const f32x4*>(lds + L_Q + u * 128 + 8 * c + 4);
    }
    qp.x = cvtpk(s0.x, s0.y); qp.y = cvtpk(s0.z, s0.w); qp.z = cvtpk(s1.x, s1.y); qp.w = cvtpk(s1.z, s1.w);
  }
  SB();
  STAMP(2);
  float sc[4] = {0.f, 0.f, 0.f, 0.f};
#pragma unroll 1
  for (int it = 0; it < NIT; ++it) {
    const bool last = it == NIT - 1;
    const __amdgpu_buffer_rsrc_t rn = last ? rv : rk;
    const int so = last ? 0 : (it + 1) * (W / 4) * TSTR;
    float xs[16];
    sfor<W>([&](auto I) {
      constexpr int i = I.v;
      float d = dot2(a[i].x, qp.x, 0.f); d = dot2(a[i].y, qp.y, d); d = dot2(a[i].z, qp.z, d); d = dot2(a[i].w, qp.w, d);
      PIN(d);
      xs[i & 15] = d;
      SB();
      a[i] = ldnt(rn, voff, i, so);
      if constexpr ((i & 15) == 15) {
#pragma unroll
        for (int u = 0; u < 8; ++u) { const float t1 = xs[u] + dppf<XOR8>(xs[u]), t2 = xs[u + 8] + dppf<XOR8>(xs[u + 8]); xs[u] = (c & 8) ? t2 : t1; }
#pragma unroll
        for (int u = 0; u < 4; ++u) { const float t1 = xs[u] + dppf<XOR7>(xs[u]), t2 = xs[u + 4] + dppf<XOR7>(xs[u + 4]); xs[u] = (c & 4) ? t2 : t1; }
#pragma unroll
        for (int u = 0; u < 2; ++u) { const float t1 = xs[u] + dppf<XOR2>(xs[u]), t2 = xs[u + 2] + dppf<XOR2>(xs[u + 2]); xs[u] = (c & 2) ? t2 : t1; }
        const float t1 = xs[0] + dppf<XOR1>(xs[0]), t2 = xs[1] + dppf<XOR1>(xs[1]);
        sc[0] = sc[1]; sc[1] = sc[2]; sc[2] = sc[3]; sc[3] = (c & 1) ? t2 : t1;
      }
      SB();
    });
  }
  STAMP(3);
  {
    const float mw = wave_max(fmaxf(fmaxf(sc[0], sc[1]), fmaxf(sc[2], sc[3])));
    if (lane == 0) lds[L_ML + w] = mw;
  }
  __syncthreads();
  float M;
  {
    const f32x4 m0 = *reinterpret_cast<const f32x4*>(lds + L_ML), m1 = *reinterpret_cast<const f32x4*>(lds + L_ML + 4);
    M = fmaxf(fmaxf(fmaxf(m0.x, m0.y), fmaxf(m0.z, m0.w)), fmaxf(fmaxf(m1.x, m1.y), fmaxf(m1.z, m1.w)));
  }
  unsigned pk[4];
  {
    float l = 0.f;
#pragma unroll
    for (int r = 0; r < 4; ++r) { const float p = __builtin_amdgcn_exp2f((sc[r] - M) * C2); l += p; pk[r] = split_hilo(p); }
    l = wave_sum(l);
    if (lane == 0) lds[L_ML + 8 + w] = l;
  }
  SB();
  STAMP(4);
  f32x4 oa = {0.f, 0.f, 0.f, 0.f};
#pragma unroll 1
  for (int it = 0; it < NIT - 1; ++it) {
    const int so = (it + 1) * (W / 4) * TSTR;
    sfor<W>([&](auto I) {
      constexpr int i = I.v;
      oa = mfma(diagA<i & 15>(pk[i >> 4], sel), a[i], oa);
      PIN(oa);
      SB();
      a[i] = ldnt(rv, voff, i, so);
      SB();
    });
#pragma unroll
    for (int r = 0; r + GPI < 4; ++r) pk[r] = pk[r + GPI];
  }
  sfor<W>([&](auto I) { oa = mfma(diagA<I.v & 15>(pk[I.v >> 4], sel), a[I.v], oa); PIN(oa); SB(); });
  STAMP(5);
  *reinterpret_cast<f32x4*>(lds + L_O + (2 * w + (g >> 1)) * OB_STR + 8 * c + 4 * (g & 1)) = oa;
  __syncthreads();
  unsigned op;
  {
    const float* src = lds + L_O + (4 * (c & 3)) * OB_STR + 16 * w + (c & 12) + g;
    float o = (src[0] + src[OB_STR]) + (src[2 * OB_STR] + src[3 * OB_STR]);
    o += dppf<XOR1>(o); o += dppf<XOR2>(o);
    op = split_hilo(o);
  }
  {
    f32x4 y[4] = {};
    sfor<4>([&](auto I) {
      const v4u A = diagA<4 * I.v>(op, sel);
      sfor<4>([&](auto N) { y[N.v] = mfma(A, wr[I.v * 4 + N.v], y[N.v]); });
    });
    sfor<2>([&](auto P) {
      f32x4 t;
#pragma unroll
      for (int r = 0; r < 4; ++r) {
        const auto s = __builtin_amdgcn_permlane32_swap(__float_as_uint(y[2 * P.v][r]), __float_as_uint(y[2 * P.v + 1][r]), false, false);
        t[r] = __uint_as_float(s[0]) + __uint_as_float(s[1]);
      }
      *reinterpret_cast<f32x4*>(lds + L_Y + w * 512 + 128 * (2 * P.v + (g >> 1)) + 8 * c + 4 * (g & 1)) = t;
    });
  }
  __syncthreads();
  const unsigned t8 = tag8 & 0xffu;
  const int j = h * 2 + half;
  const __amdgpu_buffer_rsrc_t rg = __builtin_amdgcn_make_buffer_rsrc((void*)ws, 0, WS_END, RSRC_W3);
  {
    float y = lds[L_Y + tid];
#pragma unroll
    for (int u = 1; u < NWAVE; ++u) y += lds[L_Y + u * 512 + tid];
    const unsigned yb = (__float_as_uint(y) & 0xffffff00u) | t8;
    const unsigned yn = dppu<XOR1>(yb);
    if (!(lane & 1)) __builtin_amdgcn_raw_buffer_store_b128(v4u{yb, tag32, yn, tag32}, rg, WS_Y + (b * 64 + j) * 4096 + tid * 8, 0, AUX_SC1);
    if (tid == 0) {
      const f32x4 l0 = *reinterpret_cast<const f32x4*>(lds + L_ML + 8), l1 = *reinterpret_cast<const f32x4*>(lds + L_ML + 12);
      const float L = ((l0.x + l0.y) + (l0.z + l0.w)) + ((l1.x + l1.y) + (l1.z + l1.w));
      __builtin_amdgcn_raw_buffer_store_b128(v4u{(__float_as_uint(M) & 0xffffff00u) | t8, tag32, (__float_as_uint(L) & 0xffffff00u) | t8, tag32}, rg, WS_ML + (b * 64 + j) * 16, 0, AUX_SC1);
    }
  }
  STAMP(6);
  {
    const int pl = (lane >> 2) & 7, cp = lane & 3, prod = b * 64 + 8 * w + pl;
    const unsigned char* src = ws + (lane < 32 ? WS_Y + prod * 4096 + j * 64 + cp * 16 : WS_ML + prod * 16);
    v4u gv;
#pragma unroll 1
    for (unsigned spins = 0;; ++spins) {
      asm volatile("global_load_dwordx4 %0, %1, off sc1\n\ts_waitcnt vmcnt(0)" : "=&v"(gv) : "v"(src) : "memory");
      const bool ok = gv.y == tag32 && gv.w == tag32 && (gv.x & 0xffu) == t8 && (gv.z & 0xffu) == t8;
      if (__all(ok)) break;
      if (spins > SPIN_CAP) { gv.x = gv.z = 0x7fc00000u; break; }
      __builtin_amdgcn_s_sleep(POLL_SLEEP);
    }
    const float v0 = __uint_as_float(gv.x & 0xffffff00u), v1 = __uint_as_float(gv.z & 0xffffff00u);
    const float Mo = dppf<XOR7>(v0), Lo = dppf<XOR7>(v1);
    const float Mh = fmaxf(v0, Mo), e0 = __builtin_amdgcn_exp2f((v0 - Mh) * C2), e1 = __builtin_amdgcn_exp2f((Mo - Mh) * C2);
    const float cw = e0 / (e0 * v1 + e1 * Lo);
    const auto sw = __builtin_amdgcn_permlane32_swap(__float_as_uint(cw), __float_as_uint(cw), false, false);
    const float cl = __uint_as_float(sw[1]);
    float r0 = cl * v0, r1 = cl * v1;
    r0 += dppf<ROR4>(r0); r1 += dppf<ROR4>(r1);
    r0 += dppf<XOR8>(r0); r1 += dppf<XOR8>(r1);
    if (lane < 32 && (lane & 12) == 0) *reinterpret_cast<f32x2*>(lds + L_R + (2 * w + g) * 8 + 2 * cp) = f32x2{r0, r1};
    __syncthreads();
    if (tid < 8) {
      float o = lds[L_R + tid];
#pragma unroll
      for (int u = 1; u < 16; ++u) o += lds[L_R + u * 8 + tid];
      out[b * DM + 8 * j + tid] = (uint16_t)(cvtpk(o, 0.f) & 0xffffu);
      if (tid == 0 && j == 0) __hip_atomic_store((gu64*)(ws + WS_EPOCH + b * 256), ((unsigned long long)tag8 << 32) | tag32, __ATOMIC_RELAXED, __HIP_MEMORY_SCOPE_AGENT);
    }
  }
}

extern "C" void kernel_launch(void* const* d_in, const int* in_sizes, int n_in, void* d_out, int out_size, void* d_ws, size_t ws_size, hipStream_t stream) {
  (void)in_sizes; (void)n_in; (void)out_size; (void)ws_size;
  (void)hipFuncSetAttribute((const void*)attn_fused, hipFuncAttributeMaxDynamicSharedMemorySize, LDS_BYTES);
  attn_fused<<<NWG, NTHR, LDS_BYTES, stream>>>((const uint16_t*)d_in[0], (const uint16_t*)d_in[1], (const uint16_t*)d_in[2], (const uint16_t*)d_in[3], (const uint16_t*)d_in[4],
                                               (unsigned char*)d_ws, (uint16_t*)d_out);
}
```

```cpp
#define NP 1
#include <hip/hip_runtime.h>
#include <hip/hip_bf16.h>
#include <cstdint>

typedef unsigned v4u __attribute__((ext_vector_type(4)));
typedef float f32x4 __attribute__((ext_vector_type(4)));
typedef float f32x2 __attribute__((ext_vector_type(2)));
typedef short bf16x8 __attribute__((ext_vector_type(8)));
typedef __bf16 bf16x2 __attribute__((ext_vector_type(2)));
typedef __attribute__((address_space(1))) unsigned long long gu64;

#define WIN 16
#define PREK 0
#define POLL_SLEEP 4
constexpr int DM = 512, NH = 32, SK = 4096, DH = 128;
constexpr int NWG = 256, NWAVE = 8, NTHR = NWAVE * 64;
constexpr int KEYS_WG = SK / 2;
constexpr int NLD = KEYS_WG / NWAVE / 4;
constexpr int W = WIN, NIT = NLD / W, GPI = W / 16;
constexpr int TSTR = NWAVE * 4096;
constexpr float C2 = 0.088388347648318440f * 1.4426950408889634f;
constexpr int RSRC_W3 = 0x00020000, AUX_NT = 2, AUX_SC1 = 16;
constexpr unsigned SPIN_CAP = 1u << 18;
constexpr int WS_EPOCH = 0, WS_ML = 4096, WS_Y = 8192, WS_END = WS_Y + NWG * 4096;

__device__ __forceinline__ f32x4 mfma(v4u a, v4u b, f32x4 c) {
  return __builtin_amdgcn_mfma_f32_16x16x32_bf16(__builtin_bit_cast(bf16x8, a), __builtin_bit_cast(bf16x8, b), c, 0, 0, 0);
}
__device__ __forceinline__ float dot2(unsigned k, unsigned q, float acc) {
  return __builtin_amdgcn_fdot2_f32_bf16(__builtin_bit_cast(bf16x2, k), __builtin_bit_cast(bf16x2, q), acc, false);
}
template <int CTRL> __device__ __forceinline__ unsigned dppu(unsigned x) { return (unsigned)__builtin_amdgcn_mov_dpp((int)x, CTRL, 0xf, 0xf, true); }
template <int CTRL> __device__ __forceinline__ float dppf(float x) { return __builtin_bit_cast(float, dppu<CTRL>(__builtin_bit_cast(unsigned, x))); }
constexpr int XOR1 = 0xB1, XOR2 = 0x4E, XOR7 = 0x141, XOR8 = 0x128, ROR4 = 0x124, BCAST = 0x150;
__device__ __forceinline__ unsigned cvtpk(float lo, float hi) {
  bf16x2 r = {(__bf16)lo, (__bf16)hi};
  return __builtin_bit_cast(unsigned, r);
}
__device__ __forceinline__ unsigned split_hilo(float x) {
  const unsigned h = cvtpk(x, 0.f);
  return cvtpk(__builtin_bit_cast(float, h << 16), x - __builtin_bit_cast(float, h << 16));
}
__device__ __forceinline__ float wave_max(float x) {
  x = fmaxf(x, dppf<XOR1>(x)); x = fmaxf(x, dppf<XOR2>(x)); x = fmaxf(x, dppf<XOR7>(x)); x = fmaxf(x, dppf<XOR8>(x));
  auto s = __builtin_amdgcn_permlane16_swap(__float_as_uint(x), __float_as_uint(x), false, false);
  x = fmaxf(__uint_as_float(s[0]), __uint_as_float(s[1]));
  auto t = __builtin_amdgcn_permlane32_swap(__float_as_uint(x), __float_as_uint(x), false, false);
  return fmaxf(__uint_as_float(t[0]), __uint_as_float(t[1]));
}
__device__ __forceinline__ float wave_sum(float x) {
  x += dppf<XOR1>(x); x += dppf<XOR2>(x); x += dppf<XOR7>(x); x += dppf<XOR8>(x);
  auto s = __builtin_amdgcn_permlane16_swap(__float_as_uint(x), __float_as_uint(x), false, false);
  x = __uint_as_float(s[0]) + __uint_as_float(s[1]);
  auto t = __builtin_amdgcn_permlane32_swap(__float_as_uint(x), __float_as_uint(x), false, false);
  return __uint_as_float(t[0]) + __uint_as_float(t[1]);
}
template <int SRC> __device__ __forceinline__ v4u diagA(unsigned pk, const v4u& sel) {
  const unsigned t = dppu<BCAST + SRC>(pk);
  v4u a;
  a.x = __builtin_amdgcn_perm(t, 0u, sel.x); a.y = __builtin_amdgcn_perm(t, 0u, sel.y);
  a.z = __builtin_amdgcn_perm(t, 0u, sel.z); a.w = __builtin_amdgcn_perm(t, 0u, sel.w);
  return a;
}

constexpr int OB_STR = 132;
constexpr int L_Q = 0;
constexpr int L_ML = L_Q + NWAVE * 128;
constexpr int L_O = L_ML + 32;
constexpr int L_Y = L_O + 16 * OB_STR;
constexpr int L_R = L_Y + NWAVE * 512;
constexpr int L_END = L_R + 128;
constexpr int LDS_BYTES = 84 * 1024;
static_assert(L_END * 4 <= LDS_BYTES, "lds");

#define STAMP(k) do {} while (0)
#define SB() __builtin_amdgcn_sched_barrier(0)
#define PIN(x) asm volatile("" : "+v"(x) :: "memory")
template <int I> struct IC { static constexpr int v = I; };
template <int N, int I = 0, typename F> __device__ __forceinline__ void sfor(F&& f) {
  if constexpr (I < N) { f(IC<I>{}); sfor<N, I + 1>(f); }
}
__device__ __forceinline__ v4u ldnt(__amdgpu_buffer_rsrc_t r, int voff, int i, int soff) {
  return __builtin_amdgcn_raw_buffer_load_b128(r, voff + (i & 3) * 1024, soff + (i >> 2) * TSTR, AUX_NT);
}

__global__ void __launch_bounds__(NTHR, 2) attn_fused(const uint16_t* __restrict__ x, const uint16_t* __restrict__ kc, const uint16_t* __restrict__ vc,
                                                      const uint16_t* __restrict__ wq, const uint16_t* __restrict__ wo, unsigned char* ws, uint16_t* out) {
  extern __shared__ __attribute__((aligned(16))) float lds[];
  STAMP(0);
  const int tid = threadIdx.x, lane = tid & 63, w = __builtin_amdgcn_readfirstlane(tid >> 6), g = lane >> 4, c = lane & 15;
  const int bid = blockIdx.x, h = (bid & 7) * 4 + (bid >> 6), sub = (bid >> 3) & 7, b = sub >> 1, half = sub & 1;
  const size_t kvoff = ((size_t)(b * NH + h) * SK + half * KEYS_WG) * DH + w * 16 * DH;
  const int kvbytes = KEYS_WG * DH * 2 - w * 4096;
  const __amdgpu_buffer_rsrc_t rk = __builtin_amdgcn_make_buffer_rsrc((void*)(kc + kvoff), 0, kvbytes, RSRC_W3);
  const __amdgpu_buffer_rsrc_t rv = __builtin_amdgcn_make_buffer_rsrc((void*)(vc + kvoff), 0, kvbytes, RSRC_W3);
  const __amdgpu_buffer_rsrc_t rq = __builtin_amdgcn_make_buffer_rsrc((void*)(wq + (size_t)(64 * w) * (NH * DH) + h * DH), 0, 63 * NH * DH * 2 + 256, RSRC_W3);
  const __amdgpu_buffer_rsrc_t ro = __builtin_amdgcn_make_buffer_rsrc((void*)(wo + (size_t)(h * DH + 16 * w) * DM), 0, 16 * DM * 2, RSRC_W3);
  const int voff = lane * 16;
  unsigned char* ws0 = ws;
#pragma unroll 1
  for (int pass = 0; pass < NP; ++pass) {
  ws = ws0 + (size_t)pass * (2u << 20);
  __builtin_amdgcn_s_setprio(3);
  const unsigned xv = x[b * DM + 64 * w + 4 * c + g];
  const unsigned long long epoch = __hip_atomic_load((gu64*)(ws + WS_EPOCH + b * 256), __ATOMIC_RELAXED, __HIP_MEMORY_SCOPE_AGENT);
  v4u a[W], wr[16];
  sfor<PREK>([&](auto I) { a[I.v] = ldnt(rk, voff, I.v, 0); });
  SB();
  sfor<16>([&](auto I) { wr[I.v] = __builtin_amdgcn_raw_buffer_load_b128(rq, g * (NH * DH * 2) + c * 16, I.v * 4 * (NH * DH * 2), 0); SB(); });
  __builtin_amdgcn_s_setprio(0);
  sfor<W - PREK>([&](auto I) { a[PREK + I.v] = ldnt(rk, voff, PREK + I.v, 0); SB(); });
  v4u sel;
  {
    const unsigned src = (c < 8) ? 0x0504u : 0x0706u;
    const unsigned full = (c & 1) ? (src << 16 | 0x0c0cu) : (0x0c0c0000u | src);
    const int d = (c & 7) >> 1;
    sel.x = d == 0 ? full : 0x0c0c0c0cu; sel.y = d == 1 ? full : 0x0c0c0c0cu; sel.z = d == 2 ? full : 0x0c0c0c0cu; sel.w = d == 3 ? full : 0x0c0c0c0cu;
  }
  {
    f32x4 qa = {0.f, 0.f, 0.f, 0.f};
    sfor<16>([&](auto I) { qa = mfma(diagA<I.v>(xv, sel), wr[I.v], qa); PIN(qa); });
    if (g < 2) *reinterpret_cast<f32x4*>(lds + L_Q + w * 128 + 8 * c + 4 * g) = qa;
  }
  const unsigned tag32 = __builtin_amdgcn_readfirstlane((unsigned)(epoch + 1)), tag8 = __builtin_amdgcn_readfirstlane((unsigned)((epoch + 1) >> 32));
  SB();
  STAMP(1);
  sfor<16>([&](auto I) { wr[I.v] = __builtin_amdgcn_raw_buffer_load_b128(ro, g * (DM * 2) + c * 16 + (I.v & 3) * 256, (I.v >> 2) * 4 * (DM * 2), 0); SB(); });
  __syncthreads();
  v4u qp;
  {
    f32x4 s0 = *reinterpret_cast<const f32x4*>(lds + L_Q + 8 * c), s1 = *reinterpret_cast<const f32x4*>(lds + L_Q + 8 * c + 4);
#pragma unroll
    for (int u = 1; u < NWAVE; ++u) {
      s0 += *reinterpret_cast<const f32x4*>(lds + L_Q + u * 128 + 8 * c);
      s1 += *reinterpret_cast<const f32x4*>(lds + L_Q + u * 128 + 8 * c + 4);
    }
    qp.x = cvtpk(s0.x, s0.y); qp.y = cvtpk(s0.z, s0.w); qp.z = cvtpk(s1.x, s1.y); qp.w = cvtpk(s1.z, s1.w);
  }
  SB();
  STAMP(2);
  float sc[4] = {0.f, 0.f, 0.f, 0.f};
#pragma unroll 1
  for (int it = 0; it < NIT; ++it) {
    const bool last = it == NIT - 1;
    const __amdgpu_buffer_rsrc_t rn = last ? rv : rk;
    const int so = last ? 0 : (it + 1) * (W / 4) * TSTR;
    float xs[16];
    sfor<W>([&](auto I) {
      constexpr int i = I.v;
      float d = dot2(a[i].x, qp.x, 0.f); d = dot2(a[i].y, qp.y, d); d = dot2(a[i].z, qp.z, d); d = dot2(a[i].w, qp.w, d);
      PIN(d);
      xs[i & 15] = d;
      SB();
      a[i] = ldnt(rn, voff, i, so);
      if constexpr ((i & 15) == 15) {
#pragma unroll
        for (int u = 0; u < 8; ++u) { const float t1 = xs[u] + dppf<XOR8>(xs[u]), t2 = xs[u + 8] + dppf<XOR8>(xs[u + 8]); xs[u] = (c & 8) ? t2 : t1; }
#pragma unroll
        for (int u = 0; u < 4; ++u) { const float t1 = xs[u] + dppf<XOR7>(xs[u]), t2 = xs[u + 4] + dppf<XOR7>(xs[u + 4]); xs[u] = (c & 4) ? t2 : t1; }
#pragma unroll
        for (int u = 0; u < 2; ++u) { const float t1 = xs[u] + dppf<XOR2>(xs[u]), t2 = xs[u + 2] + dppf<XOR2>(xs[u + 2]); xs[u] = (c & 2) ? t2 : t1; }
        const float t1 = xs[0] + dppf<XOR1>(xs[0]), t2 = xs[1] + dppf<XOR1>(xs[1]);
        sc[0] = sc[1]; sc[1] = sc[2]; sc[2] = sc[3]; sc[3] = (c & 1) ? t2 : t1;
      }
      SB();
    });
  }
  STAMP(3);
  {
    const float mw = wave_max(fmaxf(fmaxf(sc[0], sc[1]), fmaxf(sc[2], sc[3])));
    if (lane == 0) lds[L_ML + w] = mw;
  }
  __syncthreads();
  float M;
  {
    const f32x4 m0 = *reinterpret_cast<const f32x4*>(lds + L_ML), m1 = *reinterpret_cast<const f32x4*>(lds + L_ML + 4);
    M = fmaxf(fmaxf(fmaxf(m0.x, m0.y), fmaxf(m0.z, m0.w)), fmaxf(fmaxf(m1.x, m1.y), fmaxf(m1.z, m1.w)));
  }
  unsigned pk[4];
  {
    float l = 0.f;
#pragma unroll
    for (int r = 0; r < 4; ++r) { const float p = __builtin_amdgcn_exp2f((sc[r] - M) * C2); l += p; pk[r] = split_hilo(p); }
    l = wave_sum(l);
    if (lane == 0) lds[L_ML + 8 + w] = l;
  }
  SB();
  STAMP(4);
  f32x4 oa = {0.f, 0.f, 0.f, 0.f};
#pragma unroll 1
  for (int it = 0; it < NIT - 1; ++it) {
    const int so = (it + 1) * (W / 4) * TSTR;
    sfor<W>([&](auto I) {
      constexpr int i = I.v;
      oa = mfma(diagA<i & 15>(pk[i >> 4], sel), a[i], oa);
      PIN(oa);
      SB();
      a[i] = ldnt(rv, voff, i, so);
      SB();
    });
#pragma unroll
    for (int r = 0; r + GPI < 4; ++r) pk[r] = pk[r + GPI];
  }
  sfor<W>([&](auto I) { oa = mfma(diagA<I.v & 15>(pk[I.v >> 4], sel), a[I.v], oa); PIN(oa); SB(); });
  STAMP(5);
  *reinterpret_cast<f32x4*>(lds + L_O + (2 * w + (g >> 1)) * OB_STR + 8 * c + 4 * (g & 1)) = oa;
  __syncthreads();
  unsigned op;
  {
    const float* src = lds + L_O + (4 * (c & 3)) * OB_STR + 16 * w + (c & 12) + g;
    float o = (src[0] + src[OB_STR]) + (src[2 * OB_STR] + src[3 * OB_STR]);
    o += dppf<XOR1>(o); o += dppf<XOR2>(o);
    op = split_hilo(o);
  }
  {
    f32x4 y[4] = {};
    sfor<4>([&](auto I) {
      const v4u A = diagA<4 * I.v>(op, sel);
      sfor<4>([&](auto N) { y[N.v] = mfma(A, wr[I.v * 4 + N.v], y[N.v]); });
    });
    sfor<2>([&](auto P) {
      f32x4 t;
#pragma unroll
      for (int r = 0; r < 4; ++r) {
        const auto s = __builtin_amdgcn_permlane32_swap(__float_as_uint(y[2 * P.v][r]), __float_as_uint(y[2 * P.v + 1][r]), false, false);
        t[r] = __uint_as_float(s[0]) + __uint_as_float(s[1]);
      }
      *reinterpret_cast<f32x4*>(lds + L_Y + w * 512 + 128 * (2 * P.v + (g >> 1)) + 8 * c + 4 * (g & 1)) = t;
    });
  }
  __syncthreads();
  const unsigned t8 = tag8 & 0xffu;
  const int j = h * 2 + half;
  const __amdgpu_buffer_rsrc_t rg = __builtin_amdgcn_make_buffer_rsrc((void*)ws, 0, WS_END, RSRC_W3);
  {
    float y = lds[L_Y + tid];
#pragma unroll
    for (int u = 1; u < NWAVE; ++u) y += lds[L_Y + u * 512 + tid];
    const unsigned yb = (__float_as_uint(y) & 0xffffff00u) | t8;
    const unsigned yn = dppu<XOR1>(yb);
    if (!(lane & 1)) __builtin_amdgcn_raw_buffer_store_b128(v4u{yb, tag32, yn, tag32}, rg, WS_Y + (b * 64 + j) * 4096 + tid * 8, 0, AUX_SC1);
    if (tid == 0) {
      const f32x4 l0 = *reinterpret_cast<const f32x4*>(lds + L_ML + 8), l1 = *reinterpret_cast<const f32x4*>(lds + L_ML + 12);
      const float L = ((l0.x + l0.y) + (l0.z + l0.w)) + ((l1.x + l1.y) + (l1.z + l1.w));
      __builtin_amdgcn_raw_buffer_store_b128(v4u{(__float_as_uint(M) & 0xffffff00u) | t8, tag32, (__float_as_uint(L) & 0xffffff00u) | t8, tag32}, rg, WS_ML + (b * 64 + j) * 16, 0, AUX_SC1);
    }
  }
  STAMP(6);
  {
    const int pl = (lane >> 2) & 7, cp = lane & 3, prod = b * 64 + 8 * w + pl;
    const unsigned char* src = ws + (lane < 32 ? WS_Y + prod * 4096 + j * 64 + cp * 16 : WS_ML + prod * 16);
    v4u gv;
#pragma unroll 1
    for (unsigned spins = 0;; ++spins) {
      asm volatile("global_load_dwordx4 %0, %1, off sc1\n\ts_waitcnt vmcnt(0)" : "=&v"(gv) : "v"(src) : "memory");
      const bool ok = gv.y == tag32 && gv.w == tag32 && (gv.x & 0xffu) == t8 && (gv.z & 0xffu) == t8;
      if (__all(ok)) break;
      if (spins > SPIN_CAP) { gv.x = gv.z = 0x7fc00000u; break; }
      __builtin_amdgcn_s_sleep(POLL_SLEEP);
    }
    const float v0 = __uint_as_float(gv.x & 0xffffff00u), v1 = __uint_as_float(gv.z & 0xffffff00u);
    const float Mo = dppf<XOR7>(v0), Lo = dppf<XOR7>(v1);
    const float Mh = fmaxf(v0, Mo), e0 = __builtin_amdgcn_exp2f((v0 - Mh) * C2), e1 = __builtin_amdgcn_exp2f((Mo - Mh) * C2);
    const float cw = e0 / (e0 * v1 + e1 * Lo);
    const auto sw = __builtin_amdgcn_permlane32_swap(__float_as_uint(cw), __float_as_uint(cw), false, false);
    const float cl = __uint_as_float(sw[1]);
    float r0 = cl * v0, r1 = cl * v1;
    r0 += dppf<ROR4>(r0); r1 += dppf<ROR4>(r1);
    r0 += dppf<XOR8>(r0); r1 += dppf<XOR8>(r1);
    if (lane < 32 && (lane & 12) == 0) *reinterpret_cast<f32x2*>(lds + L_R + (2 * w + g) * 8 + 2 * cp) = f32x2{r0, r1};
    __syncthreads();
    if (tid < 8) {
      float o = lds[L_R + tid];
#pragma unroll
      for (int u = 1; u < 16; ++u) o += lds[L_R + u * 8 + tid];
      out[b * DM + 8 * j + tid] = (uint16_t)(cvtpk(o, 0.f) & 0xffffu);
      if (tid == 0 && j == 0) __hip_atomic_store((gu64*)(ws + WS_EPOCH + b * 256), ((unsigned long long)tag8 << 32) | tag32, __ATOMIC_RELAXED, __HIP_MEMORY_SCOPE_AGENT);
    }
  }
  __syncthreads();
  }
}

extern "C" void kernel_launch(void* const* d_in, const int* in_sizes, int n_in, void* d_out, int out_size, void* d_ws, size_t ws_size, hipStream_t stream) {
  (void)in_sizes; (void)n_in; (void)out_size; (void)ws_size;
  (void)hipFuncSetAttribute((const void*)attn_fused, hipFuncAttributeMaxDynamicSharedMemorySize, LDS_BYTES);
  attn_fused<<<NWG, NTHR, LDS_BYTES, stream>>>((const uint16_t*)d_in[0], (const uint16_t*)d_in[1], (const uint16_t*)d_in[2], (const uint16_t*)d_in[3], (const uint16_t*)d_in[4],
                                               (unsigned char*)d_ws, (uint16_t*)d_out);
}
```

```cpp
#define DBID 0
#define XEXPR 0ull
#include <hip/hip_runtime.h>
#include <hip/hip_bf16.h>
#include <cstdint>

typedef unsigned v4u __attribute__((ext_vector_type(4)));
typedef float f32x4 __attribute__((ext_vector_type(4)));
typedef float f32x2 __attribute__((ext_vector_type(2)));
typedef short bf16x8 __attribute__((ext_vector_type(8)));
typedef __bf16 bf16x2 __attribute__((ext_vector_type(2)));
typedef __attribute__((address_space(1))) unsigned long long gu64;

#define WIN 16
#define PREK 0
#define POLL_SLEEP 4
constexpr int DM = 512, NH = 32, SK = 4096, DH = 128;
constexpr int NWG = 256, NWAVE = 8, NTHR = NWAVE * 64;
constexpr int KEYS_WG = SK / 2;
constexpr int NLD = KEYS_WG / NWAVE / 4;
constexpr int W = WIN, NIT = NLD / W, GPI = W / 16;
constexpr int TSTR = NWAVE * 4096;
constexpr float C2 = 0.088388347648318440f * 1.4426950408889634f;
constexpr int RSRC_W3 = 0x00020000, AUX_NT = 2, AUX_SC1 = 16;
constexpr unsigned SPIN_CAP = 1u << 18;
constexpr int WS_EPOCH = 0, WS_ML = 4096, WS_Y = 8192, WS_END = WS_Y + NWG * 4096;

__device__ __forceinline__ f32x4 mfma(v4u a, v4u b, f32x4 c) {
  return __builtin_amdgcn_mfma_f32_16x16x32_bf16(__builtin_bit_cast(bf16x8, a), __builtin_bit_cast(bf16x8, b), c, 0, 0, 0);
}
__device__ __forceinline__ float dot2(unsigned k, unsigned q, float acc) {
  return __builtin_amdgcn_fdot2_f32_bf16(__builtin_bit_cast(bf16x2, k), __builtin_bit_cast(bf16x2, q), acc, false);
}
template <int CTRL> __device__ __forceinline__ unsigned dppu(unsigned x) { return (unsigned)__builtin_amdgcn_mov_dpp((int)x, CTRL, 0xf, 0xf, true); }
template <int CTRL> __device__ __forceinline__ float dppf(float x) { return __builtin_bit_cast(float, dppu<CTRL>(__builtin_bit_cast(unsigned, x))); }
constexpr int XOR1 = 0xB1, XOR2 = 0x4E, XOR7 = 0x141, XOR8 = 0x128, ROR4 = 0x124, BCAST = 0x150;
__device__ __forceinline__ unsigned cvtpk(float lo, float hi) {
  bf16x2 r = {(__bf16)lo, (__bf16)hi};
  return __builtin_bit_cast(unsigned, r);
}
__device__ __forceinline__ unsigned split_hilo(float x) {
  const unsigned h = cvtpk(x, 0.f);
  return cvtpk(__builtin_bit_cast(float, h << 16), x - __builtin_bit_cast(float, h << 16));
}
__device__ __forceinline__ float wave_max(float x) {
  x = fmaxf(x, dppf<XOR1>(x)); x = fmaxf(x, dppf<XOR2>(x)); x = fmaxf(x, dppf<XOR7>(x)); x = fmaxf(x, dppf<XOR8>(x));
  auto s = __builtin_amdgcn_permlane16_swap(__float_as_uint(x), __float_as_uint(x), false, false);
  x = fmaxf(__uint_as_float(s[0]), __uint_as_float(s[1]));
  auto t = __builtin_amdgcn_permlane32_swap(__float_as_uint(x), __float_as_uint(x), false, false);
  return fmaxf(__uint_as_float(t[0]), __uint_as_float(t[1]));
}
__device__ __forceinline__ float wave_sum(float x) {
  x += dppf<XOR1>(x); x += dppf<XOR2>(x); x += dppf<XOR7>(x); x += dppf<XOR8>(x);
  auto s = __builtin_amdgcn_permlane16_swap(__float_as_uint(x), __float_as_uint(x), false, false);
  x = __uint_as_float(s[0]) + __uint_as_float(s[1]);
  auto t = __builtin_amdgcn_permlane32_swap(__float_as_uint(x), __float_as_uint(x), false, false);
  return __uint_as_float(t[0]) + __uint_as_float(t[1]);
}
template <int SRC> __device__ __forceinline__ v4u diagA(unsigned pk, const v4u& sel) {
  const unsigned t = dppu<BCAST + SRC>(pk);
  v4u a;
  a.x = __builtin_amdgcn_perm(t, 0u, sel.x); a.y = __builtin_amdgcn_perm(t, 0u, sel.y);
  a.z = __builtin_amdgcn_perm(t, 0u, sel.z); a.w = __builtin_amdgcn_perm(t, 0u, sel.w);
  return a;
}

constexpr int OB_STR = 132;
constexpr int L_Q = 0;
constexpr int L_ML = L_Q + NWAVE * 128;
constexpr int L_O = L_ML + 32;
constexpr int L_Y = L_O + 16 * OB_STR;
constexpr int L_R = L_Y + NWAVE * 512;
constexpr int L_END = L_R + 128;
constexpr int LDS_BYTES = 84 * 1024;
static_assert(L_END * 4 <= LDS_BYTES, "lds");

#define STAMP(k) do { __builtin_amdgcn_sched_barrier(0); st_[k] = __builtin_amdgcn_s_memrealtime(); __builtin_amdgcn_sched_barrier(0); } while (0)
#define SB() __builtin_amdgcn_sched_barrier(0)
#define PIN(x) asm volatile("" : "+v"(x) :: "memory")
template <int I> struct IC { static constexpr int v = I; };
template <int N, int I = 0, typename F> __device__ __forceinline__ void sfor(F&& f) {
  if constexpr (I < N) { f(IC<I>{}); sfor<N, I + 1>(f); }
}
__device__ __forceinline__ v4u ldnt(__amdgpu_buffer_rsrc_t r, int voff, int i, int soff) {
  return __builtin_amdgcn_raw_buffer_load_b128(r, voff + (i & 3) * 1024, soff + (i >> 2) * TSTR, AUX_NT);
}

__global__ void __launch_bounds__(NTHR, 2) attn_fused(const uint16_t* __restrict__ x, const uint16_t* __restrict__ kc, const uint16_t* __restrict__ vc,
                                                      const uint16_t* __restrict__ wq, const uint16_t* __restrict__ wo, unsigned char* ws, uint16_t* out) {
  extern __shared__ __attribute__((aligned(16))) float lds[];
  unsigned long long st_[8] = {};
  STAMP(0);
  const int tid = threadIdx.x, lane = tid & 63, w = __builtin_amdgcn_readfirstlane(tid >> 6), g = lane >> 4, c = lane & 15;
  const int bid = blockIdx.x, h = (bid & 7) * 4 + (bid >> 6), sub = (bid >> 3) & 7, b = sub >> 1, half = sub & 1;
  const size_t kvoff = ((size_t)(b * NH + h) * SK + half * KEYS_WG) * DH + w * 16 * DH;
  const int kvbytes = KEYS_WG * DH * 2 - w * 4096;
  const __amdgpu_buffer_rsrc_t rk = __builtin_amdgcn_make_buffer_rsrc((void*)(kc + kvoff), 0, kvbytes, RSRC_W3);
  const __amdgpu_buffer_rsrc_t rv = __builtin_amdgcn_make_buffer_rsrc((void*)(vc + kvoff), 0, kvbytes, RSRC_W3);
  const __amdgpu_buffer_rsrc_t rq = __builtin_amdgcn_make_buffer_rsrc((void*)(wq + (size_t)(64 * w) * (NH * DH) + h * DH), 0, 63 * NH * DH * 2 + 256, RSRC_W3);
  const __amdgpu_buffer_rsrc_t ro = __builtin_amdgcn_make_buffer_rsrc((void*)(wo + (size_t)(h * DH + 16 * w) * DM), 0, 16 * DM * 2, RSRC_W3);
  const int voff = lane * 16;
  __builtin_amdgcn_s_setprio(3);
  const unsigned xv = x[b * DM + 64 * w + 4 * c + g];
  const unsigned long long epoch = __hip_atomic_load((gu64*)(ws + WS_EPOCH + b * 256), __ATOMIC_RELAXED, __HIP_MEMORY_SCOPE_AGENT);
  v4u a[W], wr[16];
  unsigned long long tka; { __builtin_amdgcn_sched_barrier(0); tka = __builtin_amdgcn_s_memrealtime(); __builtin_amdgcn_sched_barrier(0); }
  sfor<PREK>([&](auto I) { a[I.v] = ldnt(rk, voff, I.v, 0); });
  SB();
  sfor<16>([&](auto I) { wr[I.v] = __builtin_amdgcn_raw_buffer_load_b128(rq, g * (NH * DH * 2) + c * 16, I.v * 4 * (NH * DH * 2), 0); SB(); });
  __builtin_amdgcn_s_setprio(0);
  sfor<W - PREK>([&](auto I) { a[PREK + I.v] = ldnt(rk, voff, PREK + I.v, 0); SB(); });
  v4u sel;
  {
    const unsigned src = (c < 8) ? 0x0504u : 0x0706u;
    const unsigned full = (c & 1) ? (src << 16 | 0x0c0cu) : (0x0c0c0000u | src);
    const int d = (c & 7) >> 1;
    sel.x = d == 0 ? full : 0x0c0c0c0cu; sel.y = d == 1 ? full : 0x0c0c0c0cu; sel.z = d == 2 ? full : 0x0c0c0c0cu; sel.w = d == 3 ? full : 0x0c0c0c0cu;
  }
  {
    f32x4 qa = {0.f, 0.f, 0.f, 0.f};
    sfor<16>([&](auto I) { qa = mfma(diagA<I.v>(xv, sel), wr[I.v], qa); PIN(qa); });
    if (g < 2) *reinterpret_cast<f32x4*>(lds + L_Q + w * 128 + 8 * c + 4 * g) = qa;
  }
  const unsigned tag32 = __builtin_amdgcn_readfirstlane((unsigned)(epoch + 1)), tag8 = __builtin_amdgcn_readfirstlane((unsigned)((epoch + 1) >> 32));
  SB();
  STAMP(1);
  sfor<16>([&](auto I) { wr[I.v] = __builtin_amdgcn_raw_buffer_load_b128(ro, g * (DM * 2) + c * 16 + (I.v & 3) * 256, (I.v >> 2) * 4 * (DM * 2), 0); SB(); });
  __syncthreads();
  v4u qp;
  {
    f32x4 s0 = *reinterpret_cast<const f32x4*>(lds + L_Q + 8 * c), s1 = *reinterpret_cast<const f32x4*>(lds + L_Q + 8 * c + 4);
#pragma unroll
    for (int u = 1; u < NWAVE; ++u) {
      s0 += *reinterpret_cast<const f32x4*>(lds + L_Q + u * 128 + 8 * c);
      s1 += *reinterpret_cast<const f32x4*>(lds + L_Q + u * 128 + 8 * c + 4);
    }
    qp.x = cvtpk(s0.x, s0.y); qp.y = cvtpk(s0.z, s0.w); qp.z = cvtpk(s1.x, s1.y); qp.w = cvtpk(s1.z, s1.w);
  }
  SB();
  STAMP(2);
  float sc[4] = {0.f, 0.f, 0.f, 0.f};
#pragma unroll 1
  for (int it = 0; it < NIT; ++it) {
    const bool last = it == NIT - 1;
    const __amdgpu_buffer_rsrc_t rn = last ? rv : rk;
    const int so = last ? 0 : (it + 1) * (W / 4) * TSTR;
    float xs[16];
    sfor<W>([&](auto I) {
      constexpr int i = I.v;
      float d = dot2(a[i].x, qp.x, 0.f); d = dot2(a[i].y, qp.y, d); d = dot2(a[i].z, qp.z, d); d = dot2(a[i].w, qp.w, d);
      PIN(d);
      xs[i & 15] = d;
      SB();
      a[i] = ldnt(rn, voff, i, so);
      if constexpr ((i & 15) == 15) {
#pragma unroll
        for (int u = 0; u < 8; ++u) { const float t1 = xs[u] + dppf<XOR8>(xs[u]), t2 = xs[u + 8] + dppf<XOR8>(xs[u + 8]); xs[u] = (c & 8) ? t2 : t1; }
#pragma unroll
        for (int u = 0; u < 4; ++u) { const float t1 = xs[u] + dppf<XOR7>(xs[u]), t2 = xs[u + 4] + dppf<XOR7>(xs[u + 4]); xs[u] = (c & 4) ? t2 : t1; }
#pragma unroll
        for (int u = 0; u < 2; ++u) { const float t1 = xs[u] + dppf<XOR2>(xs[u]), t2 = xs[u + 2] + dppf<XOR2>(xs[u + 2]); xs[u] = (c & 2) ? t2 : t1; }
        const float t1 = xs[0] + dppf<XOR1>(xs[0]), t2 = xs[1] + dppf<XOR1>(xs[1]);
        sc[0] = sc[1]; sc[1] = sc[2]; sc[2] = sc[3]; sc[3] = (c & 1) ? t2 : t1;
      }
      SB();
    });
  }
  STAMP(3);
  {
    const float mw = wave_max(fmaxf(fmaxf(sc[0], sc[1]), fmaxf(sc[2], sc[3])));
    if (lane == 0) lds[L_ML + w] = mw;
  }
  __syncthreads();
  float M;
  {
    const f32x4 m0 = *reinterpret_cast<const f32x4*>(lds + L_ML), m1 = *reinterpret_cast<const f32x4*>(lds + L_ML + 4);
    M = fmaxf(fmaxf(fmaxf(m0.x, m0.y), fmaxf(m0.z, m0.w)), fmaxf(fmaxf(m1.x, m1.y), fmaxf(m1.z, m1.w)));
  }
  unsigned pk[4];
  {
    float l = 0.f;
#pragma unroll
    for (int r = 0; r < 4; ++r) { const float p = __builtin_amdgcn_exp2f((sc[r] - M) * C2); l += p; pk[r] = split_hilo(p); }
    l = wave_sum(l);
    if (lane == 0) lds[L_ML + 8 + w] = l;
  }
  SB();
  STAMP(4);
  f32x4 oa = {0.f, 0.f, 0.f, 0.f};
#pragma unroll 1
  for (int it = 0; it < NIT - 1; ++it) {
    const int so = (it + 1) * (W / 4) * TSTR;
    sfor<W>([&](auto I) {
      constexpr int i = I.v;
      oa = mfma(diagA<i & 15>(pk[i >> 4], sel), a[i], oa);
      PIN(oa);
      SB();
      a[i] = ldnt(rv, voff, i, so);
      SB();
    });
#pragma unroll
    for (int r = 0; r + GPI < 4; ++r) pk[r] = pk[r + GPI];
  }
  sfor<W>([&](auto I) { oa = mfma(diagA<I.v & 15>(pk[I.v >> 4], sel), a[I.v], oa); PIN(oa); SB(); });
  STAMP(5);
  *reinterpret_cast<f32x4*>(lds + L_O + (2 * w + (g >> 1)) * OB_STR + 8 * c + 4 * (g & 1)) = oa;
  __syncthreads();
  unsigned op;
  {
    const float* src = lds + L_O + (4 * (c & 3)) * OB_STR + 16 * w + (c & 12) + g;
    float o = (src[0] + src[OB_STR]) + (src[2 * OB_STR] + src[3 * OB_STR]);
    o += dppf<XOR1>(o); o += dppf<XOR2>(o);
    op = split_hilo(o);
  }
  {
    f32x4 y[4] = {};
    sfor<4>([&](auto I) {
      const v4u A = diagA<4 * I.v>(op, sel);
      sfor<4>([&](auto N) { y[N.v] = mfma(A, wr[I.v * 4 + N.v], y[N.v]); });
    });
    sfor<2>([&](auto P) {
      f32x4 t;
#pragma unroll
      for (int r = 0; r < 4; ++r) {
        const auto s = __builtin_amdgcn_permlane32_swap(__float_as_uint(y[2 * P.v][r]), __float_as_uint(y[2 * P.v + 1][r]), false, false);
        t[r] = __uint_as_float(s[0]) + __uint_as_float(s[1]);
      }
      *reinterpret_cast<f32x4*>(lds + L_Y + w * 512 + 128 * (2 * P.v + (g >> 1)) + 8 * c + 4 * (g & 1)) = t;
    });
  }
  __syncthreads();
  const unsigned t8 = tag8 & 0xffu;
  const int j = h * 2 + half;
  const __amdgpu_buffer_rsrc_t rg = __builtin_amdgcn_make_buffer_rsrc((void*)ws, 0, WS_END, RSRC_W3);
  {
    float y = lds[L_Y + tid];
#pragma unroll
    for (int u = 1; u < NWAVE; ++u) y += lds[L_Y + u * 512 + tid];
    const unsigned yb = (__float_as_uint(y) & 0xffffff00u) | t8;
    const unsigned yn = dppu<XOR1>(yb);
    if (!(lane & 1)) __builtin_amdgcn_raw_buffer_store_b128(v4u{yb, tag32, yn, tag32}, rg, WS_Y + (b * 64 + j) * 4096 + tid * 8, 0, AUX_SC1);
    if (tid == 0) {
      const f32x4 l0 = *reinterpret_cast<const f32x4*>(lds + L_ML + 8), l1 = *reinterpret_cast<const f32x4*>(lds + L_ML + 12);
      const float L = ((l0.x + l0.y) + (l0.z + l0.w)) + ((l1.x + l1.y) + (l1.z + l1.w));
      __builtin_amdgcn_raw_buffer_store_b128(v4u{(__float_as_uint(M) & 0xffffff00u) | t8, tag32, (__float_as_uint(L) & 0xffffff00u) | t8, tag32}, rg, WS_ML + (b * 64 + j) * 16, 0, AUX_SC1);
    }
  }
  STAMP(6);
  {
    const int pl = (lane >> 2) & 7, cp = lane & 3, prod = b * 64 + 8 * w + pl;
    const unsigned char* src = ws + (lane < 32 ? WS_Y + prod * 4096 + j * 64 + cp * 16 : WS_ML + prod * 16);
    v4u gv;
#pragma unroll 1
    for (unsigned spins = 0;; ++spins) {
      asm volatile("global_load_dwordx4 %0, %1, off sc1\n\ts_waitcnt vmcnt(0)" : "=&v"(gv) : "v"(src) : "memory");
      const bool ok = gv.y == tag32 && gv.w == tag32 && (gv.x & 0xffu) == t8 && (gv.z & 0xffu) == t8;
      if (__all(ok)) break;
      if (spins > SPIN_CAP) { gv.x = gv.z = 0x7fc00000u; break; }
      __builtin_amdgcn_s_sleep(POLL_SLEEP);
    }
    const float v0 = __uint_as_float(gv.x & 0xffffff00u), v1 = __uint_as_float(gv.z & 0xffffff00u);
    const float Mo = dppf<XOR7>(v0), Lo = dppf<XOR7>(v1);
    const float Mh = fmaxf(v0, Mo), e0 = __builtin_amdgcn_exp2f((v0 - Mh) * C2), e1 = __builtin_amdgcn_exp2f((Mo - Mh) * C2);
    const float cw = e0 / (e0 * v1 + e1 * Lo);
    const auto sw = __builtin_amdgcn_permlane32_swap(__float_as_uint(cw), __float_as_uint(cw), false, false);
    const float cl = __uint_as_float(sw[1]);
    float r0 = cl * v0, r1 = cl * v1;
    r0 += dppf<ROR4>(r0); r1 += dppf<ROR4>(r1);
    r0 += dppf<XOR8>(r0); r1 += dppf<XOR8>(r1);
    if (lane < 32 && (lane & 12) == 0) *reinterpret_cast<f32x2*>(lds + L_R + (2 * w + g) * 8 + 2 * cp) = f32x2{r0, r1};
    __syncthreads();
    if (tid < 8) {
      float o = lds[L_R + tid];
#pragma unroll
      for (int u = 1; u < 16; ++u) o += lds[L_R + u * 8 + tid];
      { const __amdgpu_buffer_rsrc_t rout = __builtin_amdgcn_make_buffer_rsrc((void*)out, 0, 4096, RSRC_W3); __builtin_amdgcn_raw_buffer_store_b16((unsigned short)(cvtpk(o, 0.f) & 0xffffu), rout, (b * DM + 8 * j + tid) * 2, 0, 17); }
      if (tid == 0 && j == 0) __hip_atomic_store((gu64*)(ws + WS_EPOCH + b * 256), ((unsigned long long)tag8 << 32) | tag32, __ATOMIC_RELAXED, __HIP_MEMORY_SCOPE_AGENT);
    }
  }
  STAMP(7);
  if (bid == DBID && w == 0) {
    const unsigned long long X = XEXPR;
    const unsigned long long until = st_[0] + 10000ull + X;
    while (__builtin_amdgcn_s_memrealtime() < until) __builtin_amdgcn_s_sleep(1);
  }
}

extern "C" void kernel_launch(void* const* d_in, const int* in_sizes, int n_in, void* d_out, int out_size, void* d_ws, size_t ws_size, hipStream_t stream) {
  (void)in_sizes; (void)n_in; (void)out_size; (void)ws_size;
  (void)hipFuncSetAttribute((const void*)attn_fused, hipFuncAttributeMaxDynamicSharedMemorySize, LDS_BYTES);
  attn_fused<<<NWG, NTHR, LDS_BYTES, stream>>>((const uint16_t*)d_in[0], (const uint16_t*)d_in[1], (const uint16_t*)d_in[2], (const uint16_t*)d_in[3], (const uint16_t*)d_in[4],
                                               (unsigned char*)d_ws, (uint16_t*)d_out);
}
```

```cpp
#include <hip/hip_runtime.h>
#include <hip/hip_bf16.h>
#include <cstdint>

typedef unsigned v4u __attribute__((ext_vector_type(4)));
typedef unsigned v2u __attribute__((ext_vector_type(2)));
typedef float f32x4 __attribute__((ext_vector_type(4)));
typedef short bf16x8 __attribute__((ext_vector_type(8)));
typedef short s16x4 __attribute__((ext_vector_type(4)));
typedef __bf16 bf16x2 __attribute__((ext_vector_type(2)));
typedef __attribute__((address_space(1))) unsigned long long gu64;
#define LAS __attribute__((address_space(3)))

#define XMASK_C 0x0004000020000000ull
#define TW_C 0x0004020140201008ull
constexpr int DM = 512, NH = 32, SK = 4096, DH = 128;
constexpr int NWG = 256, NWAVE = 8, NTHR = NWAVE * 64;
constexpr int KEYS_WG = SK / 2;
constexpr int W = 32;
constexpr float C2 = 0.088388347648318440f * 1.4426950408889634f;
constexpr int RSRC_W3 = 0x00020000, AUX_NT = 2, AUX_SC1 = 16;
constexpr unsigned SPIN_CAP = 1u << 18;
constexpr int WS_EPOCH = 0, WS_ML = 4096, WS_Y = 8192, WS_END = WS_Y + NWG * 4096;

__device__ __forceinline__ f32x4 mfma(v4u a, v4u b, f32x4 c) {
  return __builtin_amdgcn_mfma_f32_16x16x32_bf16(__builtin_bit_cast(bf16x8, a), __builtin_bit_cast(bf16x8, b), c, 0, 0, 0);
}
__device__ __forceinline__ float dot2(unsigned k, unsigned q, float acc) {
  return __builtin_amdgcn_fdot2_f32_bf16(__builtin_bit_cast(bf16x2, k), __builtin_bit_cast(bf16x2, q), acc, false);
}
template <int CTRL> __device__ __forceinline__ unsigned dppu(unsigned x) { return (unsigned)__builtin_amdgcn_mov_dpp((int)x, CTRL, 0xf, 0xf, true); }
template <int CTRL> __device__ __forceinline__ float dppf(float x) { return __builtin_bit_cast(float, dppu<CTRL>(__builtin_bit_cast(unsigned, x))); }
constexpr int XOR1 = 0xB1, XOR2 = 0x4E, XOR7 = 0x141, XOR8 = 0x128, ROR4 = 0x124, BCAST = 0x150;
__device__ __forceinline__ unsigned cvtpk(float lo, float hi) {
  bf16x2 r = {(__bf16)lo, (__bf16)hi};
  return __builtin_bit_cast(unsigned, r);
}
__device__ __forceinline__ unsigned split_hilo(float x) {
  const unsigned h = cvtpk(x, 0.f);
  return cvtpk(__builtin_bit_cast(float, h << 16), x - __builtin_bit_cast(float, h << 16));
}
__device__ __forceinline__ float swap16_add(float a, float b) {
  const auto s = __builtin_amdgcn_permlane16_swap(__float_as_uint(a), __float_as_uint(b), false, false);
  return __uint_as_float(s[0]) + __uint_as_float(s[1]);
}
__device__ __forceinline__ float swap32_add(float a, float b) {
  const auto s = __builtin_amdgcn_permlane32_swap(__float_as_uint(a), __float_as_uint(b), false, false);
  return __uint_as_float(s[0]) + __uint_as_float(s[1]);
}
__device__ __forceinline__ float wave_max(float x) {
  x = fmaxf(x, dppf<XOR1>(x)); x = fmaxf(x, dppf<XOR2>(x)); x = fmaxf(x, dppf<XOR7>(x)); x = fmaxf(x, dppf<XOR8>(x));
  auto s = __builtin_amdgcn_permlane16_swap(__float_as_uint(x), __float_as_uint(x), false, false);
  x = fmaxf(__uint_as_float(s[0]), __uint_as_float(s[1]));
  auto t = __builtin_amdgcn_permlane32_swap(__float_as_uint(x), __float_as_uint(x), false, false);
  return fmaxf(__uint_as_float(t[0]), __uint_as_float(t[1]));
}
__device__ __forceinline__ float wave_sum(float x) {
  x += dppf<XOR1>(x); x += dppf<XOR2>(x); x += dppf<XOR7>(x); x += dppf<XOR8>(x);
  x = swap16_add(x, x);
  return swap32_add(x, x);
}
template <int SRC> __device__ __forceinline__ v4u diagA(unsigned pk, const v4u& sel) {
  const unsigned t = dppu<BCAST + SRC>(pk);
  v4u a;
  a.x = __builtin_amdgcn_perm(t, 0u, sel.x); a.y = __builtin_amdgcn_perm(t, 0u, sel.y);
  a.z = __builtin_amdgcn_perm(t, 0u, sel.z); a.w = __builtin_amdgcn_perm(t, 0u, sel.w);
  return a;
}
__device__ __forceinline__ unsigned gran(float v, unsigned t8) { return (__float_as_uint(v) & 0xffffff00u) | t8; }
__device__ __forceinline__ float gval(unsigned x) { return __uint_as_float(x & 0xffffff00u); }
__device__ __forceinline__ unsigned gbad(const v4u& g, unsigned tag32, unsigned t8) { return (g.y ^ tag32) | (g.w ^ tag32) | (((g.x ^ t8) | (g.z ^ t8)) & 0xffu); }

constexpr int A_STR = 272;
constexpr int L_Q = 0;
constexpr int L_ML = L_Q + NWAVE * 512;
constexpr int L_A = L_ML + 64;
constexpr int L_W = L_A + 16 * A_STR;
constexpr int LDS_BYTES = L_W + NWAVE * 4096;

#define STAMP(k) do {} while (0)
#define SB() __builtin_amdgcn_sched_barrier(0)
#define PIN(x) asm volatile("" : "+v"(x) :: "memory")
template <int I> struct IC { static constexpr int v = I; };
template <int N, int I = 0, typename F> __device__ __forceinline__ void sfor(F&& f) {
  if constexpr (I < N) { f(IC<I>{}); sfor<N, I + 1>(f); }
}
__device__ __forceinline__ v4u ldnt(__amdgpu_buffer_rsrc_t r, int voff, int i, const int (&to)[8], int xoff) {
  return __builtin_amdgcn_raw_buffer_load_b128(r, voff + (i & 3) * 1024, xoff ^ to[i >> 2], AUX_NT);
}
__device__ __forceinline__ unsigned linmap(unsigned long long m, unsigned v, int nb) {
  unsigned r = 0;
#pragma unroll
  for (int i = 0; i < 8; ++i) if (i < nb) r ^= ((v >> i) & 1) ? (unsigned)(m >> (8 * i)) & 127u : 0u;
  return r;
}

__global__ void __launch_bounds__(NTHR, 2) attn_fused(const uint16_t* __restrict__ x, const uint16_t* __restrict__ kc, const uint16_t* __restrict__ vc,
                                                      const uint16_t* __restrict__ wq, const uint16_t* __restrict__ wo, unsigned char* ws, uint16_t* out
                                                      ) {
  constexpr unsigned long long XMASK = XMASK_C, TW = TW_C;
  extern __shared__ __attribute__((aligned(16))) unsigned char lds[];
  STAMP(0);
  const int tid = threadIdx.x, lane = tid & 63, w = __builtin_amdgcn_readfirstlane(tid >> 6), g = lane >> 4, c = lane & 15;
  const int bid = blockIdx.x, h = (bid & 7) * 4 + (bid >> 6), sub = (bid >> 3) & 7, b = sub >> 1, half = sub & 1;
  const int j = h * 2 + half;
  const int xw = (linmap(XMASK, bid, 8) ^ linmap(TW >> 32, w, 3)) << 12, T8 = linmap(TW, 8, 4) << 12;
  int to[8];
#pragma unroll
  for (int t = 0; t < 8; ++t) to[t] = xw ^ (linmap(TW, t, 4) << 12);
  const size_t kvoff = ((size_t)(b * NH + h) * SK + half * KEYS_WG) * DH;
  const __amdgpu_buffer_rsrc_t rk = __builtin_amdgcn_make_buffer_rsrc((void*)(kc + kvoff), 0, KEYS_WG * DH * 2, RSRC_W3);
  const __amdgpu_buffer_rsrc_t rv = __builtin_amdgcn_make_buffer_rsrc((void*)(vc + kvoff), 0, KEYS_WG * DH * 2, RSRC_W3);
  const __amdgpu_buffer_rsrc_t rq = __builtin_amdgcn_make_buffer_rsrc((void*)(wq + (size_t)(64 * w) * (NH * DH) + h * DH), 0, 63 * NH * DH * 2 + 256, RSRC_W3);
  const __amdgpu_buffer_rsrc_t ro = __builtin_amdgcn_make_buffer_rsrc((void*)(wo + (size_t)(h * DH) * DM + 64 * w), 0, 127 * DM * 2 + 128, RSRC_W3);
  const __amdgpu_buffer_rsrc_t rg = __builtin_amdgcn_make_buffer_rsrc((void*)ws, 0, WS_END, RSRC_W3);
  const int voff = lane * 16;
  unsigned long long tag = *(const __attribute__((address_space(4))) unsigned long long*)(ws + WS_EPOCH + b * 256);
  const unsigned xv = x[b * DM + 64 * w + 4 * c + g];
  v4u a[W], wr[16];
  sfor<16>([&](auto I) {
    a[16 + I.v] = __builtin_amdgcn_raw_buffer_load_b128(rq, g * (NH * DH * 2) + c * 16, I.v * 4 * (NH * DH * 2), 0); SB();
    a[I.v] = ldnt(rk, voff, I.v, to, 0); SB();
  });
  v4u sel;
  {
    const unsigned src = (c < 8) ? 0x0504u : 0x0706u;
    const unsigned full = (c & 1) ? (src << 16 | 0x0c0cu) : (0x0c0c0000u | src);
    const int d = (c & 7) >> 1;
    sel.x = d == 0 ? full : 0x0c0c0c0cu; sel.y = d == 1 ? full : 0x0c0c0c0cu; sel.z = d == 2 ? full : 0x0c0c0c0cu; sel.w = d == 3 ? full : 0x0c0c0c0cu;
  }
  {
    f32x4 qa = {0.f, 0.f, 0.f, 0.f};
    sfor<16>([&](auto I) {
      qa = mfma(diagA<I.v>(xv, sel), a[16 + I.v], qa);
      PIN(qa); SB();
      a[16 + I.v] = ldnt(rk, voff, 16 + I.v, to, 0); SB();
    });
    if (g < 2) *reinterpret_cast<f32x4*>(lds + L_Q + w * 512 + 32 * c + 16 * g) = qa;
  }
  asm volatile("" : "+s"(tag));
  tag += 1;
  const unsigned tag32 = (unsigned)tag, t8 = (unsigned)(tag >> 32) & 0xffu;
  STAMP(1);
  __syncthreads();
  v4u qp;
  {
    f32x4 s0 = *reinterpret_cast<const f32x4*>(lds + L_Q + 32 * c), s1 = *reinterpret_cast<const f32x4*>(lds + L_Q + 32 * c + 16);
#pragma unroll
    for (int u = 1; u < NWAVE; ++u) {
      s0 += *reinterpret_cast<const f32x4*>(lds + L_Q + u * 512 + 32 * c);
      s1 += *reinterpret_cast<const f32x4*>(lds + L_Q + u * 512 + 32 * c + 16);
    }
    qp.x = cvtpk(s0.x, s0.y); qp.y = cvtpk(s0.z, s0.w); qp.z = cvtpk(s1.x, s1.y); qp.w = cvtpk(s1.z, s1.w);
  }
  SB();
  STAMP(2);
  float sc[4] = {0.f, 0.f, 0.f, 0.f};
#pragma unroll 1
  for (int it = 0; it < 2; ++it) {
    const __amdgpu_buffer_rsrc_t rn = it ? rv : rk;
    const int so = it ? 0 : T8;
    float xs[16];
    sfor<W>([&](auto I) {
      constexpr int i = I.v;
      float d = dot2(a[i].x, qp.x, 0.f); d = dot2(a[i].y, qp.y, d); d = dot2(a[i].z, qp.z, d); d = dot2(a[i].w, qp.w, d);
      PIN(d);
      xs[i & 15] = d;
      SB();
      a[i] = ldnt(rn, voff, i, to, so);
      if constexpr ((i & 15) == 15) {
#pragma unroll
        for (int u = 0; u < 8; ++u) { const float t1 = xs[u] + dppf<XOR8>(xs[u]), t2 = xs[u + 8] + dppf<XOR8>(xs[u + 8]); xs[u] = (c & 8) ? t2 : t1; }
#pragma unroll
        for (int u = 0; u < 4; ++u) { const float t1 = xs[u] + dppf<XOR7>(xs[u]), t2 = xs[u + 4] + dppf<XOR7>(xs[u + 4]); xs[u] = (c & 4) ? t2 : t1; }
#pragma unroll
        for (int u = 0; u < 2; ++u) { const float t1 = xs[u] + dppf<XOR2>(xs[u]), t2 = xs[u + 2] + dppf<XOR2>(xs[u + 2]); xs[u] = (c & 2) ? t2 : t1; }
        const float t1 = xs[0] + dppf<XOR1>(xs[0]), t2 = xs[1] + dppf<XOR1>(xs[1]);
        sc[0] = sc[1]; sc[1] = sc[2]; sc[2] = sc[3]; sc[3] = (c & 1) ? t2 : t1;
        PIN(sc[3]);
      }
      SB();
    });
  }
  STAMP(3);
  {
    const float mw = wave_max(fmaxf(fmaxf(sc[0], sc[1]), fmaxf(sc[2], sc[3])));
    float l = 0.f;
#pragma unroll
    for (int r = 0; r < 4; ++r) l += __builtin_amdgcn_exp2f((sc[r] - mw) * C2);
    l = wave_sum(l);
    if (lane == 0) *reinterpret_cast<v2u*>(lds + L_ML + 8 * w) = v2u{__float_as_uint(mw), __float_as_uint(l)};
  }
  __syncthreads();
  float M, L;
  {
    const v2u ml = *reinterpret_cast<const v2u*>(lds + L_ML + 8 * (lane & 7));
    const float mu = __uint_as_float(ml.x);
    M = fmaxf(mu, dppf<XOR1>(mu)); M = fmaxf(M, dppf<XOR2>(M)); M = fmaxf(M, dppf<XOR7>(M));
    L = __uint_as_float(ml.y) * __builtin_amdgcn_exp2f((mu - M) * C2);
    L += dppf<XOR1>(L); L += dppf<XOR2>(L); L += dppf<XOR7>(L);
    if (tid == 0) __builtin_amdgcn_raw_buffer_store_b128(v4u{__float_as_uint(M), tag32, __float_as_uint(L), tag32}, rg, WS_ML + (b * 64 + j) * 16, 0, AUX_SC1);
  }
  unsigned pk[4];
#pragma unroll
  for (int r = 0; r < 4; ++r) pk[r] = split_hilo(__builtin_amdgcn_exp2f((sc[r] - M) * C2));
  SB();
  STAMP(4);
  f32x4 oa = {0.f, 0.f, 0.f, 0.f};
  sfor<W>([&](auto I) {
    constexpr int i = I.v;
    oa = mfma(diagA<i & 15>(pk[i >> 4], sel), a[i], oa);
    PIN(oa); SB();
    a[i] = ldnt(rv, voff, i, to, T8); SB();
    if constexpr (i < 16) { wr[i] = __builtin_amdgcn_raw_buffer_load_b128(ro, (lane >> 3) * (DM * 2) + (lane & 7) * 16, i * 8 * (DM * 2), 0); SB(); }
  });
  v4u mlo = __builtin_amdgcn_raw_buffer_load_b128(rg, WS_ML + (b * 64 + (j ^ 1)) * 16, 0, AUX_SC1);
  SB();
  LAS unsigned char* const wst = (LAS unsigned char*)lds + L_W + w * 4096;
  sfor<W>([&](auto I) {
    constexpr int i = I.v;
    oa = mfma(diagA<i & 15>(pk[2 + (i >> 4)], sel), a[i], oa);
    PIN(oa); SB();
    if constexpr (i >= 8 && i < 24 && (i & 3) == 0) {
      constexpr int s = (i - 8) >> 2;
      sfor<4>([&](auto U) { *(LAS v4u*)(wst + U.v * 1024 + lane * 16) = wr[4 * s + U.v]; });
      sfor<4>([&](auto T) {
        LAS unsigned char* p = wst + (8 * g + (c >> 2)) * 128 + 32 * T.v + 8 * (c & 3);
        const v2u lo = __builtin_bit_cast(v2u, __builtin_amdgcn_ds_read_tr16_b64_v4i16((LAS s16x4*)p));
        const v2u hi = __builtin_bit_cast(v2u, __builtin_amdgcn_ds_read_tr16_b64_v4i16((LAS s16x4*)(p + 512)));
        wr[4 * s + T.v] = v4u{lo.x, lo.y, hi.x, hi.y};
      });
      SB();
    }
  });
  STAMP(5);
  *reinterpret_cast<v2u*>(lds + L_A + (2 * w + (g >> 1)) * A_STR + 16 * c + 8 * (g & 1)) = v2u{cvtpk(oa.x, oa.y), cvtpk(oa.z, oa.w)};
  if (!__all(mlo.y == tag32 && mlo.w == tag32)) {
    const unsigned char* src = ws + WS_ML + (b * 64 + (j ^ 1)) * 16;
#pragma unroll 1
    for (unsigned spins = 0;; ++spins) {
      asm volatile("global_load_dwordx4 %0, %1, off sc1\n\ts_waitcnt vmcnt(0)" : "=&v"(mlo) : "v"(src) : "memory");
      if (__all(mlo.y == tag32 && mlo.w == tag32)) break;
      if (spins > SPIN_CAP) { mlo.x = mlo.z = 0x7fc00000u; break; }
      __builtin_amdgcn_s_sleep(2);
    }
  }
  float cw;
  {
    const float Mo = __uint_as_float(mlo.x), Lo = __uint_as_float(mlo.z);
    const float Mh = fmaxf(M, Mo), e0 = __builtin_amdgcn_exp2f((M - Mh) * C2), e1 = __builtin_amdgcn_exp2f((Mo - Mh) * C2);
    cw = e0 / (e0 * L + e1 * Lo);
  }
  __syncthreads();
  {
    v4u A[4];
    sfor<4>([&](auto S) { A[S.v] = *reinterpret_cast<const v4u*>(lds + L_A + c * A_STR + 64 * S.v + 16 * g); });
    SB();
    f32x4 y[4] = {};
    sfor<4>([&](auto S) { sfor<4>([&](auto T) { y[T.v] = mfma(A[S.v], wr[4 * S.v + T.v], y[T.v]); }); });
    float v[4];
    sfor<4>([&](auto T) { v[T.v] = (y[T.v].x + y[T.v].y) + (y[T.v].z + y[T.v].w); });
    const float yc = swap16_add(swap32_add(v[0], v[2]), swap32_add(v[1], v[3])) * cw;
    const unsigned yb = gran(yc, t8), yn = dppu<XOR1>(yb);
    if (!(lane & 1)) __builtin_amdgcn_raw_buffer_store_b128(v4u{yb, tag32, yn, tag32}, rg, WS_Y + (b * 64 + j) * 4096 + tid * 8, 0, AUX_SC1);
  }
  STAMP(6);
  if (w == 0) {
    const unsigned char* src = ws + WS_Y + (b * 64 + (lane >> 2)) * 4096 + j * 64 + (lane & 3) * 16;
    const unsigned char *s1 = src + 65536, *s2 = src + 2 * 65536, *s3 = src + 3 * 65536;
    v4u g0, g1, g2, g3;
#pragma unroll 1
    for (unsigned spins = 0;; ++spins) {
      asm volatile("global_load_dwordx4 %0, %4, off sc1\n\tglobal_load_dwordx4 %1, %5, off sc1\n\tglobal_load_dwordx4 %2, %6, off sc1\n\tglobal_load_dwordx4 %3, %7, off sc1\n\t"
                   "s_waitcnt vmcnt(0)"
                   : "=&v"(g0), "=&v"(g1), "=&v"(g2), "=&v"(g3) : "v"(src), "v"(s1), "v"(s2), "v"(s3) : "memory");
      if (__all((gbad(g0, tag32, t8) | gbad(g1, tag32, t8) | gbad(g2, tag32, t8) | gbad(g3, tag32, t8)) == 0)) break;
      if (spins > SPIN_CAP) { g0.x = g0.z = 0x7fc00000u; break; }
    }
    float r0 = (gval(g0.x) + gval(g1.x)) + (gval(g2.x) + gval(g3.x)), r1 = (gval(g0.z) + gval(g1.z)) + (gval(g2.z) + gval(g3.z));
    r0 += dppf<ROR4>(r0); r1 += dppf<ROR4>(r1);
    r0 += dppf<XOR8>(r0); r1 += dppf<XOR8>(r1);
    float r = swap16_add(r0, r1);
    r = swap32_add(r, r);
    if (lane < 32 && c < 4) out[b * DM + 8 * j + 2 * c + g] = (uint16_t)(cvtpk(r, 0.f) & 0xffffu);
    if (lane == 0 && j == 0) __hip_atomic_store((gu64*)(ws + WS_EPOCH + b * 256), tag, __ATOMIC_RELAXED, __HIP_MEMORY_SCOPE_AGENT);
  }
}

extern "C" void kernel_launch(void* const* d_in, const int* in_sizes, int n_in, void* d_out, int out_size, void* d_ws, size_t ws_size, hipStream_t stream) {
  (void)in_sizes; (void)n_in; (void)out_size; (void)ws_size;
  attn_fused<<<NWG, NTHR, LDS_BYTES, stream>>>((const uint16_t*)d_in[0], (const uint16_t*)d_in[1], (const uint16_t*)d_in[2], (const uint16_t*)d_in[3], (const uint16_t*)d_in[4],
                                               (unsigned char*)d_ws, (uint16_t*)d_out
    );
}
```

```cpp
#include <hip/hip_runtime.h>
#include <hip/hip_bf16.h>
#include <cstdint>

typedef unsigned v4u __attribute__((ext_vector_type(4)));
typedef unsigned v2u __attribute__((ext_vector_type(2)));
typedef float f32x4 __attribute__((ext_vector_type(4)));
typedef short bf16x8 __attribute__((ext_vector_type(8)));
typedef short s16x4 __attribute__((ext_vector_type(4)));
typedef __bf16 bf16x2 __attribute__((ext_vector_type(2)));
typedef __attribute__((address_space(1))) unsigned long long gu64;
#define LAS __attribute__((address_space(3)))

#define XMASK_C 0x0000040020000000ull
#define TW_C 0x0004020140201008ull
#define SGUESS 1
#define CLS 0
constexpr int DM = 512, NH = 32, SK = 4096, DH = 128;
constexpr int NWG = 256, NWAVE = 8, NTHR = NWAVE * 64;
constexpr int KEYS_WG = SK / 2;
constexpr int W = 32;
constexpr float C2 = 0.088388347648318440f * 1.4426950408889634f;
constexpr int RSRC_W3 = 0x00020000, AUX_NT = 2, AUX_SC1 = 16;
constexpr unsigned SPIN_CAP = 1u << 18;
constexpr int WS_EPOCH = 0, WS_ML = 4096, WS_Y = 8192, WS_END = WS_Y + NWG * 4096;

__device__ __forceinline__ f32x4 mfma(v4u a, v4u b, f32x4 c) {
  return __builtin_amdgcn_mfma_f32_16x16x32_bf16(__builtin_bit_cast(bf16x8, a), __builtin_bit_cast(bf16x8, b), c, 0, 0, 0);
}
__device__ __forceinline__ float dot2(unsigned k, unsigned q, float acc) {
  return __builtin_amdgcn_fdot2_f32_bf16(__builtin_bit_cast(bf16x2, k), __builtin_bit_cast(bf16x2, q), acc, false);
}
template <int CTRL> __device__ __forceinline__ unsigned dppu(unsigned x) { return (unsigned)__builtin_amdgcn_mov_dpp((int)x, CTRL, 0xf, 0xf, true); }
template <int CTRL> __device__ __forceinline__ float dppf(float x) { return __builtin_bit_cast(float, dppu<CTRL>(__builtin_bit_cast(unsigned, x))); }
constexpr int XOR1 = 0xB1, XOR2 = 0x4E, XOR7 = 0x141, XOR8 = 0x128, ROR4 = 0x124, BCAST = 0x150;
__device__ __forceinline__ unsigned cvtpk(float lo, float hi) {
  bf16x2 r = {(__bf16)lo, (__bf16)hi};
  return __builtin_bit_cast(unsigned, r);
}
__device__ __forceinline__ unsigned split_hilo(float x) {
  const unsigned h = cvtpk(x, 0.f);
  return cvtpk(__builtin_bit_cast(float, h << 16), x - __builtin_bit_cast(float, h << 16));
}
__device__ __forceinline__ float swap16_add(float a, float b) {
  const auto s = __builtin_amdgcn_permlane16_swap(__float_as_uint(a), __float_as_uint(b), false, false);
  return __uint_as_float(s[0]) + __uint_as_float(s[1]);
}
__device__ __forceinline__ float swap32_add(float a, float b) {
  const auto s = __builtin_amdgcn_permlane32_swap(__float_as_uint(a), __float_as_uint(b), false, false);
  return __uint_as_float(s[0]) + __uint_as_float(s[1]);
}
__device__ __forceinline__ float wave_max(float x) {
  x = fmaxf(x, dppf<XOR1>(x)); x = fmaxf(x, dppf<XOR2>(x)); x = fmaxf(x, dppf<XOR7>(x)); x = fmaxf(x, dppf<XOR8>(x));
  auto s = __builtin_amdgcn_permlane16_swap(__float_as_uint(x), __float_as_uint(x), false, false);
  x = fmaxf(__uint_as_float(s[0]), __uint_as_float(s[1]));
  auto t = __builtin_amdgcn_permlane32_swap(__float_as_uint(x), __float_as_uint(x), false, false);
  return fmaxf(__uint_as_float(t[0]), __uint_as_float(t[1]));
}
__device__ __forceinline__ float wave_sum(float x) {
  x += dppf<XOR1>(x); x += dppf<XOR2>(x); x += dppf<XOR7>(x); x += dppf<XOR8>(x);
  x = swap16_add(x, x);
  return swap32_add(x, x);
}
template <int SRC> __device__ __forceinline__ v4u diagA(unsigned pk, const v4u& sel) {
  const unsigned t = dppu<BCAST + SRC>(pk);
  v4u a;
  a.x = __builtin_amdgcn_perm(t, 0u, sel.x); a.y = __builtin_amdgcn_perm(t, 0u, sel.y);
  a.z = __builtin_amdgcn_perm(t, 0u, sel.z); a.w = __builtin_amdgcn_perm(t, 0u, sel.w);
  return a;
}
__device__ __forceinline__ unsigned gran(float v, unsigned t8) { return (__float_as_uint(v) & 0xffffff00u) | t8; }
__device__ __forceinline__ float gval(unsigned x) { return __uint_as_float(x & 0xffffff00u); }
__device__ __forceinline__ unsigned gbad(const v4u& g, unsigned tag32, unsigned t8) { return (g.y ^ tag32) | (g.w ^ tag32) | (((g.x ^ t8) | (g.z ^ t8)) & 0xffu); }

constexpr int A_STR = 272;
constexpr int L_Q = 0;
constexpr int L_ML = L_Q + NWAVE * 512;
constexpr int L_A = L_ML + 64;
constexpr int L_W = L_A + 16 * A_STR;
constexpr int LDS_BYTES = L_W + NWAVE * 4096;

#define STAMP(k) do {} while (0)
#define SB() __builtin_amdgcn_sched_barrier(0)
#define PIN(x) asm volatile("" : "+v"(x) :: "memory")
template <int I> struct IC { static constexpr int v = I; };
template <int N, int I = 0, typename F> __device__ __forceinline__ void sfor(F&& f) {
  if constexpr (I < N) { f(IC<I>{}); sfor<N, I + 1>(f); }
}
__device__ __forceinline__ v4u ldnt(__amdgpu_buffer_rsrc_t r, int voff, int i, const int (&to)[8], int xoff) {
  return __builtin_amdgcn_raw_buffer_load_b128(r, voff + (i & 3) * 1024, xoff ^ to[i >> 2], AUX_NT);
}
__device__ __forceinline__ unsigned linmap(unsigned long long m, unsigned v, int nb) {
  unsigned r = 0;
#pragma unroll
  for (int i = 0; i < 8; ++i) if (i < nb) r ^= ((v >> i) & 1) ? (unsigned)(m >> (8 * i)) & 127u : 0u;
  return r;
}

__global__ void __launch_bounds__(NTHR, 2) attn_fused(const uint16_t* __restrict__ x, const uint16_t* __restrict__ kc, const uint16_t* __restrict__ vc,
                                                      const uint16_t* __restrict__ wq, const uint16_t* __restrict__ wo, unsigned char* ws, uint16_t* out
                                                      ) {
  constexpr unsigned long long XMASK = XMASK_C, TW = TW_C;
  extern __shared__ __attribute__((aligned(16))) unsigned char lds[];
  STAMP(0);
  const int tid = threadIdx.x, lane = tid & 63, w = __builtin_amdgcn_readfirstlane(tid >> 6), g = lane >> 4, c = lane & 15;
  const int bid = blockIdx.x, h = (bid & 3) * 8 + (bid >> 5), b = (bid >> 3) & 3, half = (bid >> 2) & 1;
  const int j = h * 2 + half;
  const int cls = CLS ? (SGUESS ^ half ^ (h >> 1)) & 1 : 0;
  auto spread = [&](unsigned ti) -> int { return CLS ? (int)(((ti >> 1) << 14) | ((ti & 1) << 12)) : (int)(ti << 12); };
  const int xw = spread(linmap(XMASK, bid, 8) ^ linmap(TW >> 32, w, 3)) | (CLS ? cls << 13 : half << 19), T8 = spread(linmap(TW, 8, 4));
  int to[8];
#pragma unroll
  for (int t = 0; t < 8; ++t) to[t] = xw ^ spread(linmap(TW, t, 4));
  const size_t kvoff = (size_t)(b * NH + h) * SK * DH;
  const __amdgpu_buffer_rsrc_t rk = __builtin_amdgcn_make_buffer_rsrc((void*)(kc + kvoff), 0, SK * DH * 2, RSRC_W3);
  const __amdgpu_buffer_rsrc_t rv = __builtin_amdgcn_make_buffer_rsrc((void*)(vc + kvoff), 0, SK * DH * 2, RSRC_W3);
  const __amdgpu_buffer_rsrc_t rq = __builtin_amdgcn_make_buffer_rsrc((void*)(wq + (size_t)(64 * w) * (NH * DH) + h * DH), 0, 63 * NH * DH * 2 + 256, RSRC_W3);
  const __amdgpu_buffer_rsrc_t ro = __builtin_amdgcn_make_buffer_rsrc((void*)(wo + (size_t)(h * DH) * DM + 64 * w), 0, 127 * DM * 2 + 128, RSRC_W3);
  const __amdgpu_buffer_rsrc_t rg = __builtin_amdgcn_make_buffer_rsrc((void*)ws, 0, WS_END, RSRC_W3);
  const int voff = lane * 16;
  unsigned long long tag = *(const __attribute__((address_space(4))) unsigned long long*)(ws + WS_EPOCH + b * 256);
  const unsigned xv = x[b * DM + 64 * w + 4 * c + g];
  v4u a[W], wr[16];
  sfor<16>([&](auto I) {
    a[16 + I.v] = __builtin_amdgcn_raw_buffer_load_b128(rq, g * (NH * DH * 2) + c * 16, I.v * 4 * (NH * DH * 2), 0); SB();
    a[I.v] = ldnt(rk, voff, I.v, to, 0); SB();
  });
  v4u sel;
  {
    const unsigned src = (c < 8) ? 0x0504u : 0x0706u;
    const unsigned full = (c & 1) ? (src << 16 | 0x0c0cu) : (0x0c0c0000u | src);
    const int d = (c & 7) >> 1;
    sel.x = d == 0 ? full : 0x0c0c0c0cu; sel.y = d == 1 ? full : 0x0c0c0c0cu; sel.z = d == 2 ? full : 0x0c0c0c0cu; sel.w = d == 3 ? full : 0x0c0c0c0cu;
  }
  {
    f32x4 qa = {0.f, 0.f, 0.f, 0.f};
    sfor<16>([&](auto I) {
      qa = mfma(diagA<I.v>(xv, sel), a[16 + I.v], qa);
      PIN(qa); SB();
      a[16 + I.v] = ldnt(rk, voff, 16 + I.v, to, 0); SB();
    });
    if (g < 2) *reinterpret_cast<f32x4*>(lds + L_Q + w * 512 + 32 * c + 16 * g) = qa;
  }
  asm volatile("" : "+s"(tag));
  tag += 1;
  const unsigned tag32 = (unsigned)tag, t8 = (unsigned)(tag >> 32) & 0xffu;
  STAMP(1);
  __syncthreads();
  v4u qp;
  {
    f32x4 s0 = *reinterpret_cast<const f32x4*>(lds + L_Q + 32 * c), s1 = *reinterpret_cast<const f32x4*>(lds + L_Q + 32 * c + 16);
#pragma unroll
    for (int u = 1; u < NWAVE; ++u) {
      s0 += *reinterpret_cast<const f32x4*>(lds + L_Q + u * 512 + 32 * c);
      s1 += *reinterpret_cast<const f32x4*>(lds + L_Q + u * 512 + 32 * c + 16);
    }
    qp.x = cvtpk(s0.x, s0.y); qp.y = cvtpk(s0.z, s0.w); qp.z = cvtpk(s1.x, s1.y); qp.w = cvtpk(s1.z, s1.w);
  }
  SB();
  STAMP(2);
  float sc[4] = {0.f, 0.f, 0.f, 0.f};
#pragma unroll 1
  for (int it = 0; it < 2; ++it) {
    const __amdgpu_buffer_rsrc_t rn = it ? rv : rk;
    const int so = it ? 0 : T8;
    float xs[16];
    sfor<W>([&](auto I) {
      constexpr int i = I.v;
      float d = dot2(a[i].x, qp.x, 0.f); d = dot2(a[i].y, qp.y, d); d = dot2(a[i].z, qp.z, d); d = dot2(a[i].w, qp.w, d);
      PIN(d);
      xs[i & 15] = d;
      SB();
      a[i] = ldnt(rn, voff, i, to, so);
      if constexpr ((i & 15) == 15) {
#pragma unroll
        for (int u = 0; u < 8; ++u) { const float t1 = xs[u] + dppf<XOR8>(xs[u]), t2 = xs[u + 8] + dppf<XOR8>(xs[u + 8]); xs[u] = (c & 8) ? t2 : t1; }
#pragma unroll
        for (int u = 0; u < 4; ++u) { const float t1 = xs[u] + dppf<XOR7>(xs[u]), t2 = xs[u + 4] + dppf<XOR7>(xs[u + 4]); xs[u] = (c & 4) ? t2 : t1; }
#pragma unroll
        for (int u = 0; u < 2; ++u) { const float t1 = xs[u] + dppf<XOR2>(xs[u]), t2 = xs[u + 2] + dppf<XOR2>(xs[u + 2]); xs[u] = (c & 2) ? t2 : t1; }
        const float t1 = xs[0] + dppf<XOR1>(xs[0]), t2 = xs[1] + dppf<XOR1>(xs[1]);
        sc[0] = sc[1]; sc[1] = sc[2]; sc[2] = sc[3]; sc[3] = (c & 1) ? t2 : t1;
        PIN(sc[3]);
      }
      SB();
    });
  }
  STAMP(3);
  {
    const float mw = wave_max(fmaxf(fmaxf(sc[0], sc[1]), fmaxf(sc[2], sc[3])));
    float l = 0.f;
#pragma unroll
    for (int r = 0; r < 4; ++r) l += __builtin_amdgcn_exp2f((sc[r] - mw) * C2);
    l = wave_sum(l);
    if (lane == 0) *reinterpret_cast<v2u*>(lds + L_ML + 8 * w) = v2u{__float_as_uint(mw), __float_as_uint(l)};
  }
  __syncthreads();
  float M, L;
  {
    const v2u ml = *reinterpret_cast<const v2u*>(lds + L_ML + 8 * (lane & 7));
    const float mu = __uint_as_float(ml.x);
    M = fmaxf(mu, dppf<XOR1>(mu)); M = fmaxf(M, dppf<XOR2>(M)); M = fmaxf(M, dppf<XOR7>(M));
    L = __uint_as_float(ml.y) * __builtin_amdgcn_exp2f((mu - M) * C2);
    L += dppf<XOR1>(L); L += dppf<XOR2>(L); L += dppf<XOR7>(L);
    if (tid == 0) __builtin_amdgcn_raw_buffer_store_b128(v4u{__float_as_uint(M), tag32, __float_as_uint(L), tag32}, rg, WS_ML + (b * 64 + j) * 16, 0, AUX_SC1);
  }
  unsigned pk[4];
#pragma unroll
  for (int r = 0; r < 4; ++r) pk[r] = split_hilo(__builtin_amdgcn_exp2f((sc[r] - M) * C2));
  SB();
  STAMP(4);
  f32x4 oa = {0.f, 0.f, 0.f, 0.f};
  sfor<W>([&](auto I) {
    constexpr int i = I.v;
    oa = mfma(diagA<i & 15>(pk[i >> 4], sel), a[i], oa);
    PIN(oa); SB();
    a[i] = ldnt(rv, voff, i, to, T8); SB();
    if constexpr (i < 16) { wr[i] = __builtin_amdgcn_raw_buffer_load_b128(ro, (lane >> 3) * (DM * 2) + (lane & 7) * 16, i * 8 * (DM * 2), 0); SB(); }
  });
  v4u mlo = __builtin_amdgcn_raw_buffer_load_b128(rg, WS_ML + (b * 64 + (j ^ 1)) * 16, 0, AUX_SC1);
  SB();
  LAS unsigned char* const wst = (LAS unsigned char*)lds + L_W + w * 4096;
  sfor<W>([&](auto I) {
    constexpr int i = I.v;
    oa = mfma(diagA<i & 15>(pk[2 + (i >> 4)], sel), a[i], oa);
    PIN(oa); SB();
    if constexpr (i >= 8 && i < 24 && (i & 3) == 0) {
      constexpr int s = (i - 8) >> 2;
      sfor<4>([&](auto U) { *(LAS v4u*)(wst + U.v * 1024 + lane * 16) = wr[4 * s + U.v]; });
      sfor<4>([&](auto T) {
        LAS unsigned char* p = wst + (8 * g + (c >> 2)) * 128 + 32 * T.v + 8 * (c & 3);
        const v2u lo = __builtin_bit_cast(v2u, __builtin_amdgcn_ds_read_tr16_b64_v4i16((LAS s16x4*)p));
        const v2u hi = __builtin_bit_cast(v2u, __builtin_amdgcn_ds_read_tr16_b64_v4i16((LAS s16x4*)(p + 512)));
        wr[4 * s + T.v] = v4u{lo.x, lo.y, hi.x, hi.y};
      });
      SB();
    }
  });
  STAMP(5);
  *reinterpret_cast<v2u*>(lds + L_A + (2 * w + (g >> 1)) * A_STR + 16 * c + 8 * (g & 1)) = v2u{cvtpk(oa.x, oa.y), cvtpk(oa.z, oa.w)};
  if (!__all(mlo.y == tag32 && mlo.w == tag32)) {
    const unsigned char* src = ws + WS_ML + (b * 64 + (j ^ 1)) * 16;
#pragma unroll 1
    for (unsigned spins = 0;; ++spins) {
      asm volatile("global_load_dwordx4 %0, %1, off sc1\n\ts_waitcnt vmcnt(0)" : "=&v"(mlo) : "v"(src) : "memory");
      if (__all(mlo.y == tag32 && mlo.w == tag32)) break;
      if (spins > SPIN_CAP) { mlo.x = mlo.z = 0x7fc00000u; break; }
      __builtin_amdgcn_s_sleep(2);
    }
  }
  float cw;
  {
    const float Mo = __uint_as_float(mlo.x), Lo = __uint_as_float(mlo.z);
    const float Mh = fmaxf(M, Mo), e0 = __builtin_amdgcn_exp2f((M - Mh) * C2), e1 = __builtin_amdgcn_exp2f((Mo - Mh) * C2);
    cw = e0 / (e0 * L + e1 * Lo);
  }
  __syncthreads();
  {
    v4u A[4];
    sfor<4>([&](auto S) { A[S.v] = *reinterpret_cast<const v4u*>(lds + L_A + c * A_STR + 64 * S.v + 16 * g); });
    SB();
    f32x4 y[4] = {};
    sfor<4>([&](auto S) { sfor<4>([&](auto T) { y[T.v] = mfma(A[S.v], wr[4 * S.v + T.v], y[T.v]); }); });
    float v[4];
    sfor<4>([&](auto T) { v[T.v] = (y[T.v].x + y[T.v].y) + (y[T.v].z + y[T.v].w); });
    const float yc = swap16_add(swap32_add(v[0], v[2]), swap32_add(v[1], v[3])) * cw;
    const unsigned yb = gran(yc, t8), yn = dppu<XOR1>(yb);
    if (!(lane & 1)) __builtin_amdgcn_raw_buffer_store_b128(v4u{yb, tag32, yn, tag32}, rg, WS_Y + (b * 64 + j) * 4096 + tid * 8, 0, AUX_SC1);
  }
  STAMP(6);
  if (w == 0) {
    const unsigned char* src = ws + WS_Y + (b * 64 + (lane >> 2)) * 4096 + j * 64 + (lane & 3) * 16;
    const unsigned char *s1 = src + 65536, *s2 = src + 2 * 65536, *s3 = src + 3 * 65536;
    v4u g0, g1, g2, g3;
#pragma unroll 1
    for (unsigned spins = 0;; ++spins) {
      asm volatile("global_load_dwordx4 %0, %4, off sc1\n\tglobal_load_dwordx4 %1, %5, off sc1\n\tglobal_load_dwordx4 %2, %6, off sc1\n\tglobal_load_dwordx4 %3, %7, off sc1\n\t"
                   "s_waitcnt vmcnt(0)"
                   : "=&v"(g0), "=&v"(g1), "=&v"(g2), "=&v"(g3) : "v"(src), "v"(s1), "v"(s2), "v"(s3) : "memory");
      if (__all((gbad(g0, tag32, t8) | gbad(g1, tag32, t8) | gbad(g2, tag32, t8) | gbad(g3, tag32, t8)) == 0)) break;
      if (spins > SPIN_CAP) { g0.x = g0.z = 0x7fc00000u; break; }
    }
    float r0 = (gval(g0.x) + gval(g1.x)) + (gval(g2.x) + gval(g3.x)), r1 = (gval(g0.z) + gval(g1.z)) + (gval(g2.z) + gval(g3.z));
    r0 += dppf<ROR4>(r0); r1 += dppf<ROR4>(r1);
    r0 += dppf<XOR8>(r0); r1 += dppf<XOR8>(r1);
    float r = swap16_add(r0, r1);
    r = swap32_add(r, r);
    if (lane < 32 && c < 4) out[b * DM + 8 * j + 2 * c + g] = (uint16_t)(cvtpk(r, 0.f) & 0xffffu);
    if (lane == 0 && j == 0) __hip_atomic_store((gu64*)(ws + WS_EPOCH + b * 256), tag, __ATOMIC_RELAXED, __HIP_MEMORY_SCOPE_AGENT);
  }
}

extern "C" void kernel_launch(void* const* d_in, const int* in_sizes, int n_in, void* d_out, int out_size, void* d_ws, size_t ws_size, hipStream_t stream) {
  (void)in_sizes; (void)n_in; (void)out_size; (void)ws_size;
  attn_fused<<<NWG, NTHR, LDS_BYTES, stream>>>((const uint16_t*)d_in[0], (const uint16_t*)d_in[1], (const uint16_t*)d_in[2], (const uint16_t*)d_in[3], (const uint16_t*)d_in[4],
                                               (unsigned char*)d_ws, (uint16_t*)d_out
    );
}
```

```cpp
#include <hip/hip_runtime.h>
#include <hip/hip_bf16.h>
#include <cstdint>

typedef unsigned v4u __attribute__((ext_vector_type(4)));
typedef unsigned v2u __attribute__((ext_vector_type(2)));
typedef float f32x4 __attribute__((ext_vector_type(4)));
typedef short bf16x8 __attribute__((ext_vector_type(8)));
typedef short s16x4 __attribute__((ext_vector_type(4)));
typedef __bf16 bf16x2 __attribute__((ext_vector_type(2)));
typedef __attribute__((address_space(1))) unsigned long long gu64;
#define LAS __attribute__((address_space(3)))

constexpr int DM = 512, NH = 32, SK = 4096, DH = 128;
constexpr int NWG = 256, NWAVE = 8, NTHR = NWAVE * 64;
constexpr int KEYS_WG = SK / 2;
constexpr int W = 32;
constexpr int ROW = NWAVE * 4096;
constexpr float C2 = 0.088388347648318440f * 1.4426950408889634f;
constexpr int RSRC_W3 = 0x00020000, AUX_NT = 2, AUX_SC1 = 16;
constexpr unsigned SPIN_CAP = 1u << 18;
constexpr int WS_EPOCH = 0, WS_ML = 4096, WS_Y = 8192, WS_END = WS_Y + NWG * 4096;
constexpr int A_STR = 272;
constexpr int L_Q = 0;
constexpr int L_ML = L_Q + NWAVE * 512;
constexpr int L_A = L_ML + 64;
constexpr int L_W = L_A + 16 * A_STR;
constexpr int LDS_BYTES = L_W + NWAVE * 4096;

__device__ __forceinline__ f32x4 mfma(v4u a, v4u b, f32x4 c) {
  return __builtin_amdgcn_mfma_f32_16x16x32_bf16(__builtin_bit_cast(bf16x8, a), __builtin_bit_cast(bf16x8, b), c, 0, 0, 0);
}
__device__ __forceinline__ float dot2(unsigned k, unsigned q, float acc) {
  return __builtin_amdgcn_fdot2_f32_bf16(__builtin_bit_cast(bf16x2, k), __builtin_bit_cast(bf16x2, q), acc, false);
}
template <int CTRL> __device__ __forceinline__ unsigned dppu(unsigned x) { return (unsigned)__builtin_amdgcn_mov_dpp((int)x, CTRL, 0xf, 0xf, true); }
template <int CTRL> __device__ __forceinline__ float dppf(float x) { return __builtin_bit_cast(float, dppu<CTRL>(__builtin_bit_cast(unsigned, x))); }
constexpr int XOR1 = 0xB1, XOR2 = 0x4E, XOR7 = 0x141, XOR8 = 0x128, ROR4 = 0x124, BCAST = 0x150;
__device__ __forceinline__ unsigned cvtpk(float lo, float hi) {
  bf16x2 r = {(__bf16)lo, (__bf16)hi};
  return __builtin_bit_cast(unsigned, r);
}
__device__ __forceinline__ unsigned split_hilo(float x) {
  const unsigned h = cvtpk(x, 0.f);
  return cvtpk(__builtin_bit_cast(float, h << 16), x - __builtin_bit_cast(float, h << 16));
}
__device__ __forceinline__ float swap16_add(float a, float b) {
  const auto s = __builtin_amdgcn_permlane16_swap(__float_as_uint(a), __float_as_uint(b), false, false);
  return __uint_as_float(s[0]) + __uint_as_float(s[1]);
}
__device__ __forceinline__ float swap32_add(float a, float b) {
  const auto s = __builtin_amdgcn_permlane32_swap(__float_as_uint(a), __float_as_uint(b), false, false);
  return __uint_as_float(s[0]) + __uint_as_float(s[1]);
}
__device__ __forceinline__ float wave_max(float x) {
  x = fmaxf(x, dppf<XOR1>(x)); x = fmaxf(x, dppf<XOR2>(x)); x = fmaxf(x, dppf<XOR7>(x)); x = fmaxf(x, dppf<XOR8>(x));
  auto s = __builtin_amdgcn_permlane16_swap(__float_as_uint(x), __float_as_uint(x), false, false);
  x = fmaxf(__uint_as_float(s[0]), __uint_as_float(s[1]));
  auto t = __builtin_amdgcn_permlane32_swap(__float_as_uint(x), __float_as_uint(x), false, false);
  return fmaxf(__uint_as_float(t[0]), __uint_as_float(t[1]));
}
__device__ __forceinline__ float wave_sum(float x) {
  x += dppf<XOR1>(x); x += dppf<XOR2>(x); x += dppf<XOR7>(x); x += dppf<XOR8>(x);
  x = swap16_add(x, x);
  return swap32_add(x, x);
}
template <int SRC> __device__ __forceinline__ v4u diagA(unsigned pk, const v4u& sel) {
  const unsigned t = dppu<BCAST + SRC>(pk);
  v4u a;
  a.x = __builtin_amdgcn_perm(t, 0u, sel.x); a.y = __builtin_amdgcn_perm(t, 0u, sel.y);
  a.z = __builtin_amdgcn_perm(t, 0u, sel.z); a.w = __builtin_amdgcn_perm(t, 0u, sel.w);
  return a;
}
__device__ __forceinline__ unsigned gran(float v, unsigned t8) { return (__float_as_uint(v) & 0xffffff00u) | t8; }
__device__ __forceinline__ float gval(unsigned x) { return __uint_as_float(x & 0xffffff00u); }
__device__ __forceinline__ unsigned gbad(const v4u& g, unsigned tag32, unsigned t8) { return (g.y ^ tag32) | (g.w ^ tag32) | (((g.x ^ t8) | (g.z ^ t8)) & 0xffu); }

#define SB() __builtin_amdgcn_sched_barrier(0)
#define PIN(x) asm volatile("" : "+v"(x) :: "memory")
template <int I> struct IC { static constexpr int v = I; };
template <int N, int I = 0, typename F> __device__ __forceinline__ void sfor(F&& f) {
  if constexpr (I < N) { f(IC<I>{}); sfor<N, I + 1>(f); }
}
__device__ __forceinline__ v4u ldnt(__amdgpu_buffer_rsrc_t r, int voff, int i, int xo) {
  return __builtin_amdgcn_raw_buffer_load_b128(r, voff + (i & 3) * 1024, xo ^ ((i >> 2) * ROW), AUX_NT);
}

__global__ void __launch_bounds__(NTHR, 2) mha_decode_fused(const uint16_t* __restrict__ x, const uint16_t* __restrict__ kc, const uint16_t* __restrict__ vc,
                                                            const uint16_t* __restrict__ wq, const uint16_t* __restrict__ wo, unsigned char* ws, uint16_t* out) {
  extern __shared__ __attribute__((aligned(16))) unsigned char lds[];
  const int tid = threadIdx.x, lane = tid & 63, w = __builtin_amdgcn_readfirstlane(tid >> 6), g = lane >> 4, c = lane & 15;
  const int bid = blockIdx.x, h = (bid & 7) * 4 + (bid >> 6), sub = (bid >> 3) & 7, b = sub >> 1, half = sub & 1;
  const int j = h * 2 + half;
  const int wv = w ^ sub, ix = (sub & 3) << 2, ox = sub << 1;
  const int xw = ((w ^ ((h & 1) << 2)) << 12) | (half << 17), T8 = 8 * ROW;
  const size_t kvoff = ((size_t)(b * NH + h) * SK + half * KEYS_WG) * DH;
  const __amdgpu_buffer_rsrc_t rk = __builtin_amdgcn_make_buffer_rsrc((void*)(kc + kvoff), 0, KEYS_WG * DH * 2, RSRC_W3);
  const __amdgpu_buffer_rsrc_t rv = __builtin_amdgcn_make_buffer_rsrc((void*)(vc + kvoff), 0, KEYS_WG * DH * 2, RSRC_W3);
  const __amdgpu_buffer_rsrc_t rq = __builtin_amdgcn_make_buffer_rsrc((void*)(wq + (size_t)(64 * wv) * (NH * DH) + h * DH), 0, 63 * NH * DH * 2 + 256, RSRC_W3);
  const __amdgpu_buffer_rsrc_t ro = __builtin_amdgcn_make_buffer_rsrc((void*)(wo + (size_t)(h * DH) * DM + 64 * w), 0, 127 * DM * 2 + 128, RSRC_W3);
  const __amdgpu_buffer_rsrc_t rg = __builtin_amdgcn_make_buffer_rsrc((void*)ws, 0, WS_END, RSRC_W3);
  const int voff = lane * 16;
  const unsigned long long epoch = __hip_atomic_load((gu64*)(ws + WS_EPOCH + b * 256), __ATOMIC_RELAXED, __HIP_MEMORY_SCOPE_AGENT);
  const unsigned xv = x[b * DM + 64 * wv + 4 * (c ^ ix) + g];
  v4u a[W], wr[16];
  sfor<16>([&](auto I) {
    a[16 + I.v] = __builtin_amdgcn_raw_buffer_load_b128(rq, g * (NH * DH * 2) + c * 16, (I.v ^ ix) * 4 * (NH * DH * 2), 0); SB();
    a[I.v] = ldnt(rk, voff, I.v, xw); SB();
  });
  v4u sel;
  {
    const unsigned src = (c < 8) ? 0x0504u : 0x0706u;
    const unsigned full = (c & 1) ? (src << 16 | 0x0c0cu) : (0x0c0c0000u | src);
    const int d = (c & 7) >> 1;
    sel.x = d == 0 ? full : 0x0c0c0c0cu; sel.y = d == 1 ? full : 0x0c0c0c0cu; sel.z = d == 2 ? full : 0x0c0c0c0cu; sel.w = d == 3 ? full : 0x0c0c0c0cu;
  }
  {
    f32x4 qa = {0.f, 0.f, 0.f, 0.f};
    sfor<16>([&](auto I) {
      qa = mfma(diagA<I.v>(xv, sel), a[16 + I.v], qa);
      PIN(qa); SB();
      a[16 + I.v] = ldnt(rk, voff, 16 + I.v, xw); SB();
    });
    if (g < 2) *reinterpret_cast<f32x4*>(lds + L_Q + w * 512 + 32 * c + 16 * g) = qa;
  }
  const unsigned long long tag = ((unsigned long long)(unsigned)__builtin_amdgcn_readfirstlane((unsigned)(epoch >> 32)) << 32 |
                                  (unsigned)__builtin_amdgcn_readfirstlane((unsigned)epoch)) + 1;
  const unsigned tag32 = (unsigned)tag, t8 = (unsigned)(tag >> 32) & 0xffu;
  __syncthreads();
  v4u qp;
  {
    f32x4 s0 = *reinterpret_cast<const f32x4*>(lds + L_Q + 32 * c), s1 = *reinterpret_cast<const f32x4*>(lds + L_Q + 32 * c + 16);
#pragma unroll
    for (int u = 1; u < NWAVE; ++u) {
      s0 += *reinterpret_cast<const f32x4*>(lds + L_Q + u * 512 + 32 * c);
      s1 += *reinterpret_cast<const f32x4*>(lds + L_Q + u * 512 + 32 * c + 16);
    }
    qp.x = cvtpk(s0.x, s0.y); qp.y = cvtpk(s0.z, s0.w); qp.z = cvtpk(s1.x, s1.y); qp.w = cvtpk(s1.z, s1.w);
  }
  SB();
  float sc[4] = {0.f, 0.f, 0.f, 0.f};
#pragma unroll 1
  for (int it = 0; it < 2; ++it) {
    const __amdgpu_buffer_rsrc_t rn = it ? rv : rk;
    const int xo = it ? xw : xw ^ T8;
    float xs[16];
    sfor<W>([&](auto I) {
      constexpr int i = I.v;
      float d = dot2(a[i].x, qp.x, 0.f); d = dot2(a[i].y, qp.y, d); d = dot2(a[i].z, qp.z, d); d = dot2(a[i].w, qp.w, d);
      PIN(d);
      xs[i & 15] = d;
      SB();
      a[i] = ldnt(rn, voff, i, xo);
      if constexpr ((i & 15) == 15) {
#pragma unroll
        for (int u = 0; u < 8; ++u) { const float t1 = xs[u] + dppf<XOR8>(xs[u]), t2 = xs[u + 8] + dppf<XOR8>(xs[u + 8]); xs[u] = (c & 8) ? t2 : t1; }
#pragma unroll
        for (int u = 0; u < 4; ++u) { const float t1 = xs[u] + dppf<XOR7>(xs[u]), t2 = xs[u + 4] + dppf<XOR7>(xs[u + 4]); xs[u] = (c & 4) ? t2 : t1; }
#pragma unroll
        for (int u = 0; u < 2; ++u) { const float t1 = xs[u] + dppf<XOR2>(xs[u]), t2 = xs[u + 2] + dppf<XOR2>(xs[u + 2]); xs[u] = (c & 2) ? t2 : t1; }
        const float t1 = xs[0] + dppf<XOR1>(xs[0]), t2 = xs[1] + dppf<XOR1>(xs[1]);
        sc[0] = sc[1]; sc[1] = sc[2]; sc[2] = sc[3]; sc[3] = (c & 1) ? t2 : t1;
        PIN(sc[3]);
      }
      SB();
    });
  }
  {
    const float mw = wave_max(fmaxf(fmaxf(sc[0], sc[1]), fmaxf(sc[2], sc[3])));
    float l = 0.f;
#pragma unroll
    for (int r = 0; r < 4; ++r) l += __builtin_amdgcn_exp2f((sc[r] - mw) * C2);
    l = wave_sum(l);
    if (lane == 0) *reinterpret_cast<v2u*>(lds + L_ML + 8 * w) = v2u{__float_as_uint(mw), __float_as_uint(l)};
  }
  __syncthreads();
  float M, L;
  {
    const v2u ml = *reinterpret_cast<const v2u*>(lds + L_ML + 8 * (lane & 7));
    const float mu = __uint_as_float(ml.x);
    M = fmaxf(mu, dppf<XOR1>(mu)); M = fmaxf(M, dppf<XOR2>(M)); M = fmaxf(M, dppf<XOR7>(M));
    L = __uint_as_float(ml.y) * __builtin_amdgcn_exp2f((mu - M) * C2);
    L += dppf<XOR1>(L); L += dppf<XOR2>(L); L += dppf<XOR7>(L);
    if (tid == 0) __builtin_amdgcn_raw_buffer_store_b128(v4u{__float_as_uint(M), tag32, __float_as_uint(L), tag32}, rg, WS_ML + (b * 64 + j) * 16, 0, AUX_SC1);
  }
  unsigned pk[4];
#pragma unroll
  for (int r = 0; r < 4; ++r) pk[r] = split_hilo(__builtin_amdgcn_exp2f((sc[r] - M) * C2));
  SB();
  f32x4 oa = {0.f, 0.f, 0.f, 0.f};
  sfor<W>([&](auto I) {
    constexpr int i = I.v;
    oa = mfma(diagA<i & 15>(pk[i >> 4], sel), a[i], oa);
    PIN(oa); SB();
    a[i] = ldnt(rv, voff, i, xw ^ T8); SB();
    if constexpr (i < 16) { wr[i] = __builtin_amdgcn_raw_buffer_load_b128(ro, (lane >> 3) * (DM * 2) + (lane & 7) * 16, (i ^ ox) * 8 * (DM * 2), 0); SB(); }
  });
  v4u mlo = __builtin_amdgcn_raw_buffer_load_b128(rg, WS_ML + (b * 64 + (j ^ 1)) * 16, 0, AUX_SC1);
  SB();
  LAS unsigned char* const wst = (LAS unsigned char*)lds + L_W + w * 4096;
  int aw, ar, yo;
  sfor<W>([&](auto I) {
    constexpr int i = I.v;
    oa = mfma(diagA<i & 15>(pk[2 + (i >> 4)], sel), a[i], oa);
    PIN(oa); SB();
    if constexpr (i == 24) {
      aw = L_A + (2 * w + (g >> 1)) * A_STR + 16 * c + 8 * (g & 1); ar = L_A + c * A_STR + 16 * g; yo = WS_Y + (b * 64 + j) * 4096 + tid * 8;
      PIN(aw); PIN(ar); PIN(yo); SB();
    }
    if constexpr (i >= 8 && i < 24 && (i & 3) == 0) {
      constexpr int s = (i - 8) >> 2;
      sfor<4>([&](auto U) { *(LAS v4u*)(wst + (U.v ^ (ox & 3)) * 1024 + lane * 16) = wr[4 * s + U.v]; });
      sfor<4>([&](auto T) {
        LAS unsigned char* p = wst + (8 * g + (c >> 2)) * 128 + 32 * T.v + 8 * (c & 3);
        const v2u lo = __builtin_bit_cast(v2u, __builtin_amdgcn_ds_read_tr16_b64_v4i16((LAS s16x4*)p));
        const v2u hi = __builtin_bit_cast(v2u, __builtin_amdgcn_ds_read_tr16_b64_v4i16((LAS s16x4*)(p + 512)));
        wr[4 * s + T.v] = v4u{lo.x, lo.y, hi.x, hi.y};
      });
      SB();
    }
  });
  *reinterpret_cast<v2u*>(lds + aw) = v2u{cvtpk(oa.x, oa.y), cvtpk(oa.z, oa.w)};
  SB();
  if (!__all(mlo.y == tag32 && mlo.w == tag32)) {
    const unsigned char* src = ws + WS_ML + (b * 64 + (j ^ 1)) * 16;
#pragma unroll 1
    for (unsigned spins = 0;; ++spins) {
      asm volatile("global_load_dwordx4 %0, %1, off sc1\n\ts_waitcnt vmcnt(0)" : "=&v"(mlo) : "v"(src) : "memory");
      if (__all(mlo.y == tag32 && mlo.w == tag32)) break;
      if (spins > SPIN_CAP) { mlo.x = mlo.z = 0x7fc00000u; break; }
      __builtin_amdgcn_s_sleep(2);
    }
  }
  __syncthreads();
  {
    v4u A[4];
    sfor<4>([&](auto S) { A[S.v] = *reinterpret_cast<const v4u*>(lds + ar + 64 * (S.v ^ (ox >> 2))); });
    SB();
    PIN(mlo.x);
    const float Mo = __uint_as_float(mlo.x), Lo = __uint_as_float(mlo.z);
    const float Mh = fmaxf(M, Mo), e0 = __builtin_amdgcn_exp2f((M - Mh) * C2), e1 = __builtin_amdgcn_exp2f((Mo - Mh) * C2);
    const float cw = e0 / (e0 * L + e1 * Lo);
    f32x4 y[4] = {};
    sfor<4>([&](auto S) { sfor<4>([&](auto T) { y[T.v] = mfma(A[S.v], wr[4 * S.v + T.v], y[T.v]); }); });
    float v[4];
    sfor<4>([&](auto T) { v[T.v] = (y[T.v].x + y[T.v].y) + (y[T.v].z + y[T.v].w); });
    const float yc = swap16_add(swap32_add(v[0], v[2]), swap32_add(v[1], v[3])) * cw;
    const unsigned yb = gran(yc, t8), yn = dppu<XOR1>(yb);
    if (!(lane & 1)) __builtin_amdgcn_raw_buffer_store_b128(v4u{yb, tag32, yn, tag32}, rg, yo, 0, AUX_SC1);
  }
  if (w == 0) {
    const unsigned char* src = ws + WS_Y + (b * 64 + (lane >> 2)) * 4096 + j * 64 + (lane & 3) * 16;
    const unsigned char *s1 = src + 65536, *s2 = src + 2 * 65536, *s3 = src + 3 * 65536;
    v4u g0, g1, g2, g3;
#pragma unroll 1
    for (unsigned spins = 0;; ++spins) {
      asm volatile("global_load_dwordx4 %0, %4, off sc1\n\tglobal_load_dwordx4 %1, %5, off sc1\n\tglobal_load_dwordx4 %2, %6, off sc1\n\tglobal_load_dwordx4 %3, %7, off sc1\n\t"
                   "s_waitcnt vmcnt(0)"
                   : "=&v"(g0), "=&v"(g1), "=&v"(g2), "=&v"(g3) : "v"(src), "v"(s1), "v"(s2), "v"(s3) : "memory");
      if (__all((gbad(g0, tag32, t8) | gbad(g1, tag32, t8) | gbad(g2, tag32, t8) | gbad(g3, tag32, t8)) == 0)) break;
      if (spins > SPIN_CAP) { g0.x = g0.z = 0x7fc00000u; break; }
    }
    float r0 = (gval(g0.x) + gval(g1.x)) + (gval(g2.x) + gval(g3.x)), r1 = (gval(g0.z) + gval(g1.z)) + (gval(g2.z) + gval(g3.z));
    r0 += dppf<ROR4>(r0); r1 += dppf<ROR4>(r1);
    r0 += dppf<XOR8>(r0); r1 += dppf<XOR8>(r1);
    float r = swap16_add(r0, r1);
    r = swap32_add(r, r);
    if (lane < 32 && c < 4) out[b * DM + 8 * j + 2 * c + g] = (uint16_t)(cvtpk(r, 0.f) & 0xffffu);
    if (lane == 0 && j == 0) __hip_atomic_store((gu64*)(ws + WS_EPOCH + b * 256), tag, __ATOMIC_RELAXED, __HIP_MEMORY_SCOPE_AGENT);
  }
}

extern "C" void kernel_launch(void* const* d_in, const int* in_sizes, int n_in, void* d_out, int out_size, void* d_ws, size_t ws_size, hipStream_t stream) {
  (void)in_sizes; (void)n_in; (void)out_size; (void)ws_size;
  mha_decode_fused<<<NWG, NTHR, LDS_BYTES, stream>>>((const uint16_t*)d_in[0], (const uint16_t*)d_in[1], (const uint16_t*)d_in[2], (const uint16_t*)d_in[3],
                                                     (const uint16_t*)d_in[4], (unsigned char*)d_ws, (uint16_t*)d_out);
}
```

```cpp
#include <hip/hip_runtime.h>
#include <hip/hip_bf16.h>
#include <cstdint>

typedef unsigned v4u __attribute__((ext_vector_type(4)));
typedef unsigned v2u __attribute__((ext_vector_type(2)));
typedef float f32x4 __attribute__((ext_vector_type(4)));
typedef short bf16x8 __attribute__((ext_vector_type(8)));
typedef short s16x4 __attribute__((ext_vector_type(4)));
typedef __bf16 bf16x2 __attribute__((ext_vector_type(2)));
typedef __attribute__((address_space(1))) unsigned long long gu64;
#define LAS __attribute__((address_space(3)))

#define XMASK_C 0x0004000020000000ull
#define TW_C 0x0004020140201008ull
#define WQX 1
#define WOX 0
constexpr int DM = 512, NH = 32, SK = 4096, DH = 128;
constexpr int NWG = 256, NWAVE = 8, NTHR = NWAVE * 64;
constexpr int KEYS_WG = SK / 2;
constexpr int W = 32;
constexpr float C2 = 0.088388347648318440f * 1.4426950408889634f;
constexpr int RSRC_W3 = 0x00020000, AUX_NT = 2, AUX_SC1 = 16;
constexpr unsigned SPIN_CAP = 1u << 18;
constexpr int WS_EPOCH = 0, WS_ML = 4096, WS_Y = 8192, WS_END = WS_Y + NWG * 4096;

__device__ __forceinline__ f32x4 mfma(v4u a, v4u b, f32x4 c) {
  return __builtin_amdgcn_mfma_f32_16x16x32_bf16(__builtin_bit_cast(bf16x8, a), __builtin_bit_cast(bf16x8, b), c, 0, 0, 0);
}
__device__ __forceinline__ float dot2(unsigned k, unsigned q, float acc) {
  return __builtin_amdgcn_fdot2_f32_bf16(__builtin_bit_cast(bf16x2, k), __builtin_bit_cast(bf16x2, q), acc, false);
}
template <int CTRL> __device__ __forceinline__ unsigned dppu(unsigned x) { return (unsigned)__builtin_amdgcn_mov_dpp((int)x, CTRL, 0xf, 0xf, true); }
template <int CTRL> __device__ __forceinline__ float dppf(float x) { return __builtin_bit_cast(float, dppu<CTRL>(__builtin_bit_cast(unsigned, x))); }
constexpr int XOR1 = 0xB1, XOR2 = 0x4E, XOR7 = 0x141, XOR8 = 0x128, ROR4 = 0x124, BCAST = 0x150;
__device__ __forceinline__ unsigned cvtpk(float lo, float hi) {
  bf16x2 r = {(__bf16)lo, (__bf16)hi};
  return __builtin_bit_cast(unsigned, r);
}
__device__ __forceinline__ unsigned split_hilo(float x) {
  const unsigned h = cvtpk(x, 0.f);
  return cvtpk(__builtin_bit_cast(float, h << 16), x - __builtin_bit_cast(float, h << 16));
}
__device__ __forceinline__ float swap16_add(float a, float b) {
  const auto s = __builtin_amdgcn_permlane16_swap(__float_as_uint(a), __float_as_uint(b), false, false);
  return __uint_as_float(s[0]) + __uint_as_float(s[1]);
}
__device__ __forceinline__ float swap32_add(float a, float b) {
  const auto s = __builtin_amdgcn_permlane32_swap(__float_as_uint(a), __float_as_uint(b), false, false);
  return __uint_as_float(s[0]) + __uint_as_float(s[1]);
}
__device__ __forceinline__ float wave_max(float x) {
  x = fmaxf(x, dppf<XOR1>(x)); x = fmaxf(x, dppf<XOR2>(x)); x = fmaxf(x, dppf<XOR7>(x)); x = fmaxf(x, dppf<XOR8>(x));
  auto s = __builtin_amdgcn_permlane16_swap(__float_as_uint(x), __float_as_uint(x), false, false);
  x = fmaxf(__uint_as_float(s[0]), __uint_as_float(s[1]));
  auto t = __builtin_amdgcn_permlane32_swap(__float_as_uint(x), __float_as_uint(x), false, false);
  return fmaxf(__uint_as_float(t[0]), __uint_as_float(t[1]));
}
__device__ __forceinline__ float wave_sum(float x) {
  x += dppf<XOR1>(x); x += dppf<XOR2>(x); x += dppf<XOR7>(x); x += dppf<XOR8>(x);
  x = swap16_add(x, x);
  return swap32_add(x, x);
}
template <int SRC> __device__ __forceinline__ v4u diagA(unsigned pk, const v4u& sel) {
  const unsigned t = dppu<BCAST + SRC>(pk);
  v4u a;
  a.x = __builtin_amdgcn_perm(t, 0u, sel.x); a.y = __builtin_amdgcn_perm(t, 0u, sel.y);
  a.z = __builtin_amdgcn_perm(t, 0u, sel.z); a.w = __builtin_amdgcn_perm(t, 0u, sel.w);
  return a;
}
__device__ __forceinline__ unsigned gran(float v, unsigned t8) { return (__float_as_uint(v) & 0xffffff00u) | t8; }
__device__ __forceinline__ float gval(unsigned x) { return __uint_as_float(x & 0xffffff00u); }
__device__ __forceinline__ unsigned gbad(const v4u& g, unsigned tag32, unsigned t8) { return (g.y ^ tag32) | (g.w ^ tag32) | (((g.x ^ t8) | (g.z ^ t8)) & 0xffu); }

constexpr int A_STR = 272;
constexpr int L_Q = 0;
constexpr int L_ML = L_Q + NWAVE * 512;
constexpr int L_A = L_ML + 64;
constexpr int L_W = L_A + 16 * A_STR;
constexpr int LDS_BYTES = L_W + NWAVE * 4096;

#define STAMP(k) do {} while (0)
#define SB() __builtin_amdgcn_sched_barrier(0)
#define PIN(x) asm volatile("" : "+v"(x) :: "memory")
template <int I> struct IC { static constexpr int v = I; };
template <int N, int I = 0, typename F> __device__ __forceinline__ void sfor(F&& f) {
  if constexpr (I < N) { f(IC<I>{}); sfor<N, I + 1>(f); }
}
__device__ __forceinline__ v4u ldnt(__amdgpu_buffer_rsrc_t r, int voff, int i, const int (&to)[8], int xoff) {
  return __builtin_amdgcn_raw_buffer_load_b128(r, voff + (i & 3) * 1024, xoff ^ to[i >> 2], AUX_NT);
}
__device__ __forceinline__ unsigned linmap(unsigned long long m, unsigned v, int nb) {
  unsigned r = 0;
#pragma unroll
  for (int i = 0; i < 8; ++i) if (i < nb) r ^= ((v >> i) & 1) ? (unsigned)(m >> (8 * i)) & 127u : 0u;
  return r;
}

__global__ void __launch_bounds__(NTHR, 2) attn_fused(const uint16_t* __restrict__ x, const uint16_t* __restrict__ kc, const uint16_t* __restrict__ vc,
                                                      const uint16_t* __restrict__ wq, const uint16_t* __restrict__ wo, unsigned char* ws, uint16_t* out
                                                      ) {
  constexpr unsigned long long XMASK = XMASK_C, TW = TW_C;
  extern __shared__ __attribute__((aligned(16))) unsigned char lds[];
  STAMP(0);
  const int tid = threadIdx.x, lane = tid & 63, w = __builtin_amdgcn_readfirstlane(tid >> 6), g = lane >> 4, c = lane & 15;
  const int bid = blockIdx.x, h = (bid & 7) * 4 + (bid >> 6), sub = (bid >> 3) & 7, b = sub >> 1, half = sub & 1;
  const int j = h * 2 + half;
  const int wv = WQX ? w ^ sub : w, ix = WQX ? (sub & 3) << 2 : 0, ox = WOX ? sub << 1 : 0;
  const int xw = (linmap(XMASK, bid, 8) ^ linmap(TW >> 32, w, 3)) << 12, T8 = linmap(TW, 8, 4) << 12;
  int to[8];
#pragma unroll
  for (int t = 0; t < 8; ++t) to[t] = xw ^ (linmap(TW, t, 4) << 12);
  const size_t kvoff = ((size_t)(b * NH + h) * SK + half * KEYS_WG) * DH;
  const __amdgpu_buffer_rsrc_t rk = __builtin_amdgcn_make_buffer_rsrc((void*)(kc + kvoff), 0, KEYS_WG * DH * 2, RSRC_W3);
  const __amdgpu_buffer_rsrc_t rv = __builtin_amdgcn_make_buffer_rsrc((void*)(vc + kvoff), 0, KEYS_WG * DH * 2, RSRC_W3);
  const __amdgpu_buffer_rsrc_t rq = __builtin_amdgcn_make_buffer_rsrc((void*)(wq + (size_t)(64 * wv) * (NH * DH) + h * DH), 0, 63 * NH * DH * 2 + 256, RSRC_W3);
  const __amdgpu_buffer_rsrc_t ro = __builtin_amdgcn_make_buffer_rsrc((void*)(wo + (size_t)(h * DH) * DM + 64 * w), 0, 127 * DM * 2 + 128, RSRC_W3);
  const __amdgpu_buffer_rsrc_t rg = __builtin_amdgcn_make_buffer_rsrc((void*)ws, 0, WS_END, RSRC_W3);
  const int voff = lane * 16;
  const unsigned long long epoch = __hip_atomic_load((gu64*)(ws + WS_EPOCH + b * 256), __ATOMIC_RELAXED, __HIP_MEMORY_SCOPE_AGENT);
  const unsigned xv = x[b * DM + 64 * wv + 4 * (c ^ ix) + g];
  v4u a[W], wr[16];
  sfor<16>([&](auto I) {
    a[16 + I.v] = __builtin_amdgcn_raw_buffer_load_b128(rq, g * (NH * DH * 2) + c * 16, (I.v ^ ix) * 4 * (NH * DH * 2), 0); SB();
    a[I.v] = ldnt(rk, voff, I.v, to, 0); SB();
  });
  v4u sel;
  {
    const unsigned src = (c < 8) ? 0x0504u : 0x0706u;
    const unsigned full = (c & 1) ? (src << 16 | 0x0c0cu) : (0x0c0c0000u | src);
    const int d = (c & 7) >> 1;
    sel.x = d == 0 ? full : 0x0c0c0c0cu; sel.y = d == 1 ? full : 0x0c0c0c0cu; sel.z = d == 2 ? full : 0x0c0c0c0cu; sel.w = d == 3 ? full : 0x0c0c0c0cu;
  }
  {
    f32x4 qa = {0.f, 0.f, 0.f, 0.f};
    sfor<16>([&](auto I) {
      qa = mfma(diagA<I.v>(xv, sel), a[16 + I.v], qa);
      PIN(qa); SB();
      a[16 + I.v] = ldnt(rk, voff, 16 + I.v, to, 0); SB();
    });
    if (g < 2) *reinterpret_cast<f32x4*>(lds + L_Q + w * 512 + 32 * c + 16 * g) = qa;
  }
  const unsigned long long tag = ((unsigned long long)(unsigned)__builtin_amdgcn_readfirstlane((unsigned)(epoch >> 32)) << 32 | (unsigned)__builtin_amdgcn_readfirstlane((unsigned)epoch)) + 1;
  const unsigned tag32 = (unsigned)tag, t8 = (unsigned)(tag >> 32) & 0xffu;
  STAMP(1);
  __syncthreads();
  v4u qp;
  {
    f32x4 s0 = *reinterpret_cast<const f32x4*>(lds + L_Q + 32 * c), s1 = *reinterpret_cast<const f32x4*>(lds + L_Q + 32 * c + 16);
#pragma unroll
    for (int u = 1; u < NWAVE; ++u) {
      s0 += *reinterpret_cast<const f32x4*>(lds + L_Q + u * 512 + 32 * c);
      s1 += *reinterpret_cast<const f32x4*>(lds + L_Q + u * 512 + 32 * c + 16);
    }
    qp.x = cvtpk(s0.x, s0.y); qp.y = cvtpk(s0.z, s0.w); qp.z = cvtpk(s1.x, s1.y); qp.w = cvtpk(s1.z, s1.w);
  }
  SB();
  STAMP(2);
  float sc[4] = {0.f, 0.f, 0.f, 0.f};
#pragma unroll 1
  for (int it = 0; it < 2; ++it) {
    const __amdgpu_buffer_rsrc_t rn = it ? rv : rk;
    const int so = it ? 0 : T8;
    float xs[16];
    sfor<W>([&](auto I) {
      constexpr int i = I.v;
      float d = dot2(a[i].x, qp.x, 0.f); d = dot2(a[i].y, qp.y, d); d = dot2(a[i].z, qp.z, d); d = dot2(a[i].w, qp.w, d);
      PIN(d);
      xs[i & 15] = d;
      SB();
      a[i] = ldnt(rn, voff, i, to, so);
      if constexpr ((i & 15) == 15) {
#pragma unroll
        for (int u = 0; u < 8; ++u) { const float t1 = xs[u] + dppf<XOR8>(xs[u]), t2 = xs[u + 8] + dppf<XOR8>(xs[u + 8]); xs[u] = (c & 8) ? t2 : t1; }
#pragma unroll
        for (int u = 0; u < 4; ++u) { const float t1 = xs[u] + dppf<XOR7>(xs[u]), t2 = xs[u + 4] + dppf<XOR7>(xs[u + 4]); xs[u] = (c & 4) ? t2 : t1; }
#pragma unroll
        for (int u = 0; u < 2; ++u) { const float t1 = xs[u] + dppf<XOR2>(xs[u]), t2 = xs[u + 2] + dppf<XOR2>(xs[u + 2]); xs[u] = (c & 2) ? t2 : t1; }
        const float t1 = xs[0] + dppf<XOR1>(xs[0]), t2 = xs[1] + dppf<XOR1>(xs[1]);
        sc[0] = sc[1]; sc[1] = sc[2]; sc[2] = sc[3]; sc[3] = (c & 1) ? t2 : t1;
        PIN(sc[3]);
      }
      SB();
    });
  }
  STAMP(3);
  {
    const float mw = wave_max(fmaxf(fmaxf(sc[0], sc[1]), fmaxf(sc[2], sc[3])));
    float l = 0.f;
#pragma unroll
    for (int r = 0; r < 4; ++r) l += __builtin_amdgcn_exp2f((sc[r] - mw) * C2);
    l = wave_sum(l);
    if (lane == 0) *reinterpret_cast<v2u*>(lds + L_ML + 8 * w) = v2u{__float_as_uint(mw), __float_as_uint(l)};
  }
  __syncthreads();
  float M, L;
  {
    const v2u ml = *reinterpret_cast<const v2u*>(lds + L_ML + 8 * (lane & 7));
    const float mu = __uint_as_float(ml.x);
    M = fmaxf(mu, dppf<XOR1>(mu)); M = fmaxf(M, dppf<XOR2>(M)); M = fmaxf(M, dppf<XOR7>(M));
    L = __uint_as_float(ml.y) * __builtin_amdgcn_exp2f((mu - M) * C2);
    L += dppf<XOR1>(L); L += dppf<XOR2>(L); L += dppf<XOR7>(L);
    if (tid == 0) __builtin_amdgcn_raw_buffer_store_b128(v4u{__float_as_uint(M), tag32, __float_as_uint(L), tag32}, rg, WS_ML + (b * 64 + j) * 16, 0, AUX_SC1);
  }
  unsigned pk[4];
#pragma unroll
  for (int r = 0; r < 4; ++r) pk[r] = split_hilo(__builtin_amdgcn_exp2f((sc[r] - M) * C2));
  SB();
  STAMP(4);
  f32x4 oa = {0.f, 0.f, 0.f, 0.f};
  sfor<W>([&](auto I) {
    constexpr int i = I.v;
    oa = mfma(diagA<i & 15>(pk[i >> 4], sel), a[i], oa);
    PIN(oa); SB();
    a[i] = ldnt(rv, voff, i, to, T8); SB();
    if constexpr (i < 16) { wr[i] = __builtin_amdgcn_raw_buffer_load_b128(ro, (lane >> 3) * (DM * 2) + (lane & 7) * 16, (i ^ ox) * 8 * (DM * 2), 0); SB(); }
  });
  v4u mlo = __builtin_amdgcn_raw_buffer_load_b128(rg, WS_ML + (b * 64 + (j ^ 1)) * 16, 0, AUX_SC1);
  SB();
  LAS unsigned char* const wst = (LAS unsigned char*)lds + L_W + w * 4096;
  int aw, ar, yo;
  sfor<W>([&](auto I) {
    constexpr int i = I.v;
    oa = mfma(diagA<i & 15>(pk[2 + (i >> 4)], sel), a[i], oa);
    PIN(oa); SB();
    if constexpr (i == 24) {
      aw = L_A + (2 * w + (g >> 1)) * A_STR + 16 * c + 8 * (g & 1); ar = L_A + c * A_STR + 16 * g; yo = WS_Y + (b * 64 + j) * 4096 + tid * 8;
      PIN(aw); PIN(ar); PIN(yo); SB();
    }
    if constexpr (i >= 8 && i < 24 && (i & 3) == 0) {
      constexpr int s = (i - 8) >> 2;
      sfor<4>([&](auto U) { *(LAS v4u*)(wst + (U.v ^ (ox & 3)) * 1024 + lane * 16) = wr[4 * s + U.v]; });
      sfor<4>([&](auto T) {
        LAS unsigned char* p = wst + (8 * g + (c >> 2)) * 128 + 32 * T.v + 8 * (c & 3);
        const v2u lo = __builtin_bit_cast(v2u, __builtin_amdgcn_ds_read_tr16_b64_v4i16((LAS s16x4*)p));
        const v2u hi = __builtin_bit_cast(v2u, __builtin_amdgcn_ds_read_tr16_b64_v4i16((LAS s16x4*)(p + 512)));
        wr[4 * s + T.v] = v4u{lo.x, lo.y, hi.x, hi.y};
      });
      SB();
    }
  });
  STAMP(5);
  *reinterpret_cast<v2u*>(lds + aw) = v2u{cvtpk(oa.x, oa.y), cvtpk(oa.z, oa.w)};
  SB();
  if (!__all(mlo.y == tag32 && mlo.w == tag32)) {
    const unsigned char* src = ws + WS_ML + (b * 64 + (j ^ 1)) * 16;
#pragma unroll 1
    for (unsigned spins = 0;; ++spins) {
      asm volatile("global_load_dwordx4 %0, %1, off sc1\n\ts_waitcnt vmcnt(0)" : "=&v"(mlo) : "v"(src) : "memory");
      if (__all(mlo.y == tag32 && mlo.w == tag32)) break;
      if (spins > SPIN_CAP) { mlo.x = mlo.z = 0x7fc00000u; break; }
      __builtin_amdgcn_s_sleep(2);
    }
  }
  __syncthreads();
  {
    v4u A[4];
    sfor<4>([&](auto S) { A[S.v] = *reinterpret_cast<const v4u*>(lds + ar + 64 * (S.v ^ (ox >> 2))); });
    SB();
    PIN(mlo.x);
    float cw;
    {
      const float Mo = __uint_as_float(mlo.x), Lo = __uint_as_float(mlo.z);
      const float Mh = fmaxf(M, Mo), e0 = __builtin_amdgcn_exp2f((M - Mh) * C2), e1 = __builtin_amdgcn_exp2f((Mo - Mh) * C2);
      cw = e0 / (e0 * L + e1 * Lo);
    }
    f32x4 y[4] = {};
    sfor<4>([&](auto S) { sfor<4>([&](auto T) { y[T.v] = mfma(A[S.v], wr[4 * S.v + T.v], y[T.v]); }); });
    float v[4];
    sfor<4>([&](auto T) { v[T.v] = (y[T.v].x + y[T.v].y) + (y[T.v].z + y[T.v].w); });
    const float yc = swap16_add(swap32_add(v[0], v[2]), swap32_add(v[1], v[3])) * cw;
    const unsigned yb = gran(yc, t8), yn = dppu<XOR1>(yb);
    if (!(lane & 1)) __builtin_amdgcn_raw_buffer_store_b128(v4u{yb, tag32, yn, tag32}, rg, yo, 0, AUX_SC1);
  }
  STAMP(6);
  if (w == 0) {
    const unsigned char* src = ws + WS_Y + (b * 64 + (lane >> 2)) * 4096 + j * 64 + (lane & 3) * 16;
    const unsigned char *s1 = src + 65536, *s2 = src + 2 * 65536, *s3 = src + 3 * 65536;
    v4u g0, g1, g2, g3;
#pragma unroll 1
    for (unsigned spins = 0;; ++spins) {
      asm volatile("global_load_dwordx4 %0, %4, off sc1\n\tglobal_load_dwordx4 %1, %5, off sc1\n\tglobal_load_dwordx4 %2, %6, off sc1\n\tglobal_load_dwordx4 %3, %7, off sc1\n\t"
                   "s_waitcnt vmcnt(0)"
                   : "=&v"(g0), "=&v"(g1), "=&v"(g2), "=&v"(g3) : "v"(src), "v"(s1), "v"(s2), "v"(s3) : "memory");
      if (__all((gbad(g0, tag32, t8) | gbad(g1, tag32, t8) | gbad(g2, tag32, t8) | gbad(g3, tag32, t8)) == 0)) break;
      if (spins > SPIN_CAP) { g0.x = g0.z = 0x7fc00000u; break; }
    }
    float r0 = (gval(g0.x) + gval(g1.x)) + (gval(g2.x) + gval(g3.x)), r1 = (gval(g0.z) + gval(g1.z)) + (gval(g2.z) + gval(g3.z));
    r0 += dppf<ROR4>(r0); r1 += dppf<ROR4>(r1);
    r0 += dppf<XOR8>(r0); r1 += dppf<XOR8>(r1);
    float r = swap16_add(r0, r1);
    r = swap32_add(r, r);
    if (lane < 32 && c < 4) out[b * DM + 8 * j + 2 * c + g] = (uint16_t)(cvtpk(r, 0.f) & 0xffffu);
    if (lane == 0 && j == 0) __hip_atomic_store((gu64*)(ws + WS_EPOCH + b * 256), tag, __ATOMIC_RELAXED, __HIP_MEMORY_SCOPE_AGENT);
  }
}

extern "C" void kernel_launch(void* const* d_in, const int* in_sizes, int n_in, void* d_out, int out_size, void* d_ws, size_t ws_size, hipStream_t stream) {
  (void)in_sizes; (void)n_in; (void)out_size; (void)ws_size;
  attn_fused<<<NWG, NTHR, LDS_BYTES, stream>>>((const uint16_t*)d_in[0], (const uint16_t*)d_in[1], (const uint16_t*)d_in[2], (const uint16_t*)d_in[3], (const uint16_t*)d_in[4],
                                               (unsigned char*)d_ws, (uint16_t*)d_out
    );
}
```

```cpp
#include <hip/hip_runtime.h>
#include <hip/hip_bf16.h>
#include <cstdint>

typedef unsigned v4u __attribute__((ext_vector_type(4)));
typedef unsigned v2u __attribute__((ext_vector_type(2)));
typedef float f32x4 __attribute__((ext_vector_type(4)));
typedef short bf16x8 __attribute__((ext_vector_type(8)));
typedef short s16x4 __attribute__((ext_vector_type(4)));
typedef __bf16 bf16x2 __attribute__((ext_vector_type(2)));
typedef __attribute__((address_space(1))) unsigned long long gu64;
#define LAS __attribute__((address_space(3)))

constexpr int DM = 512, NH = 32, SK = 4096, DH = 128;
constexpr int NWG = 256, NWAVE = 8, NTHR = NWAVE * 64;
constexpr int KEYS_WG = SK / 2;
constexpr int W = 32;
constexpr int ROW = NWAVE * 4096;
constexpr float C2 = 0.088388347648318440f * 1.4426950408889634f;
constexpr int RSRC_W3 = 0x00020000, AUX_NT = 2, AUX_SC1 = 16;
constexpr unsigned SPIN_CAP = 1u << 18;
constexpr int WS_EPOCH = 0, WS_ML = 4096, WS_Y = 8192, WS_END = WS_Y + NWG * 4096;
constexpr int A_STR = 272;
constexpr int L_Q = 0;
constexpr int L_ML = L_Q + NWAVE * 512;
constexpr int L_A = L_ML + 64;
constexpr int L_W = L_A + 16 * A_STR;
constexpr int LDS_BYTES = L_W + NWAVE * 4096;

__device__ __forceinline__ f32x4 mfma(v4u a, v4u b, f32x4 c) {
  return __builtin_amdgcn_mfma_f32_16x16x32_bf16(__builtin_bit_cast(bf16x8, a), __builtin_bit_cast(bf16x8, b), c, 0, 0, 0);
}
__device__ __forceinline__ float dot2(unsigned k, unsigned q, float acc) {
  return __builtin_amdgcn_fdot2_f32_bf16(__builtin_bit_cast(bf16x2, k), __builtin_bit_cast(bf16x2, q), acc, false);
}
template <int CTRL> __device__ __forceinline__ unsigned dppu(unsigned x) { return (unsigned)__builtin_amdgcn_mov_dpp((int)x, CTRL, 0xf, 0xf, true); }
template <int CTRL> __device__ __forceinline__ float dppf(float x) { return __builtin_bit_cast(float, dppu<CTRL>(__builtin_bit_cast(unsigned, x))); }
constexpr int XOR1 = 0xB1, XOR2 = 0x4E, XOR7 = 0x141, XOR8 = 0x128, ROR4 = 0x124, BCAST = 0x150;
__device__ __forceinline__ unsigned cvtpk(float lo, float hi) {
  bf16x2 r = {(__bf16)lo, (__bf16)hi};
  return __builtin_bit_cast(unsigned, r);
}
__device__ __forceinline__ unsigned split_hilo(float x) {
  const unsigned h = cvtpk(x, 0.f);
  return cvtpk(__builtin_bit_cast(float, h << 16), x - __builtin_bit_cast(float, h << 16));
}
__device__ __forceinline__ float swap16_add(float a, float b) {
  const auto s = __builtin_amdgcn_permlane16_swap(__float_as_uint(a), __float_as_uint(b), false, false);
  return __uint_as_float(s[0]) + __uint_as_float(s[1]);
}
__device__ __forceinline__ float swap32_add(float a, float b) {
  const auto s = __builtin_amdgcn_permlane32_swap(__float_as_uint(a), __float_as_uint(b), false, false);
  return __uint_as_float(s[0]) + __uint_as_float(s[1]);
}
__device__ __forceinline__ float wave_max(float x) {
  x = fmaxf(x, dppf<XOR1>(x)); x = fmaxf(x, dppf<XOR2>(x)); x = fmaxf(x, dppf<XOR7>(x)); x = fmaxf(x, dppf<XOR8>(x));
  auto s = __builtin_amdgcn_permlane16_swap(__float_as_uint(x), __float_as_uint(x), false, false);
  x = fmaxf(__uint_as_float(s[0]), __uint_as_float(s[1]));
  auto t = __builtin_amdgcn_permlane32_swap(__float_as_uint(x), __float_as_uint(x), false, false);
  return fmaxf(__uint_as_float(t[0]), __uint_as_float(t[1]));
}
__device__ __forceinline__ float wave_sum(float x) {
  x += dppf<XOR1>(x); x += dppf<XOR2>(x); x += dppf<XOR7>(x); x += dppf<XOR8>(x);
  x = swap16_add(x, x);
  return swap32_add(x, x);
}
template <int SRC> __device__ __forceinline__ v4u diagA(unsigned pk, const v4u& sel) {
  const unsigned t = dppu<BCAST + SRC>(pk);
  v4u a;
  a.x = __builtin_amdgcn_perm(t, 0u, sel.x); a.y = __builtin_amdgcn_perm(t, 0u, sel.y);
  a.z = __builtin_amdgcn_perm(t, 0u, sel.z); a.w = __builtin_amdgcn_perm(t, 0u, sel.w);
  return a;
}
__device__ __forceinline__ unsigned gran(float v, unsigned t8) { return (__float_as_uint(v) & 0xffffff00u) | t8; }
__device__ __forceinline__ float gval(unsigned x) { return __uint_as_float(x & 0xffffff00u); }
__device__ __forceinline__ unsigned gbad(const v4u& g, unsigned tag32, unsigned t8) { return (g.y ^ tag32) | (g.w ^ tag32) | (((g.x ^ t8) | (g.z ^ t8)) & 0xffu); }

#define SB() __builtin_amdgcn_sched_barrier(0)
#define PIN(x) asm volatile("" : "+v"(x) :: "memory")
template <int I> struct IC { static constexpr int v = I; };
template <int N, int I = 0, typename F> __device__ __forceinline__ void sfor(F&& f) {
  if constexpr (I < N) { f(IC<I>{}); sfor<N, I + 1>(f); }
}
__device__ __forceinline__ v4u ldnt(__amdgpu_buffer_rsrc_t r, int voff, int i, int xo) {
  return __builtin_amdgcn_raw_buffer_load_b128(r, voff + (i & 3) * 1024, xo ^ ((i >> 2) * ROW), AUX_NT);
}

__global__ void __launch_bounds__(NTHR, 2) mha_decode_fused(const uint16_t* __restrict__ x, const uint16_t* __restrict__ kc, const uint16_t* __restrict__ vc,
                                                            const uint16_t* __restrict__ wq, const uint16_t* __restrict__ wo, unsigned char* ws, uint16_t* out) {
  extern __shared__ __attribute__((aligned(16))) unsigned char lds[];
  const int tid = threadIdx.x, lane = tid & 63, w = __builtin_amdgcn_readfirstlane(tid >> 6), g = lane >> 4, c = lane & 15;
  const int bid = blockIdx.x, h = (bid & 7) * 4 + (bid >> 6), sub = (bid >> 3) & 7, b = sub >> 1, half = sub & 1;
  const int j = h * 2 + half;
  const int wv = w ^ sub, ix = (sub & 3) << 2, ox = sub << 1;
  const int xw = (w << 12) ^ ((h & 1) << 14) ^ (half << 17), T8 = 8 * ROW;
  const size_t kvoff = ((size_t)(b * NH + h) * SK + half * KEYS_WG) * DH;
  const __amdgpu_buffer_rsrc_t rk = __builtin_amdgcn_make_buffer_rsrc((void*)(kc + kvoff), 0, KEYS_WG * DH * 2, RSRC_W3);
  const __amdgpu_buffer_rsrc_t rv = __builtin_amdgcn_make_buffer_rsrc((void*)(vc + kvoff), 0, KEYS_WG * DH * 2, RSRC_W3);
  const __amdgpu_buffer_rsrc_t rq = __builtin_amdgcn_make_buffer_rsrc((void*)(wq + (size_t)(64 * wv) * (NH * DH) + h * DH), 0, 63 * NH * DH * 2 + 256, RSRC_W3);
  const __amdgpu_buffer_rsrc_t ro = __builtin_amdgcn_make_buffer_rsrc((void*)(wo + (size_t)(h * DH) * DM + 64 * w), 0, 127 * DM * 2 + 128, RSRC_W3);
  const __amdgpu_buffer_rsrc_t rg = __builtin_amdgcn_make_buffer_rsrc((void*)ws, 0, WS_END, RSRC_W3);
  const int voff = lane * 16;
  const unsigned long long epoch = __hip_atomic_load((gu64*)(ws + WS_EPOCH + b * 256), __ATOMIC_RELAXED, __HIP_MEMORY_SCOPE_AGENT);
  const unsigned xv = x[b * DM + 64 * wv + 4 * (c ^ ix) + g];
  v4u a[W], wr[16];
  sfor<16>([&](auto I) {
    a[16 + I.v] = __builtin_amdgcn_raw_buffer_load_b128(rq, g * (NH * DH * 2) + c * 16, (I.v ^ ix) * 4 * (NH * DH * 2), 0); SB();
    a[I.v] = ldnt(rk, voff, I.v, xw); SB();
  });
  v4u sel;
  {
    const unsigned src = (c < 8) ? 0x0504u : 0x0706u;
    const unsigned full = (c & 1) ? (src << 16 | 0x0c0cu) : (0x0c0c0000u | src);
    const int d = (c & 7) >> 1;
    sel.x = d == 0 ? full : 0x0c0c0c0cu; sel.y = d == 1 ? full : 0x0c0c0c0cu; sel.z = d == 2 ? full : 0x0c0c0c0cu; sel.w = d == 3 ? full : 0x0c0c0c0cu;
  }
  {
    f32x4 qa = {0.f, 0.f, 0.f, 0.f};
    sfor<16>([&](auto I) {
      qa = mfma(diagA<I.v>(xv, sel), a[16 + I.v], qa);
      PIN(qa); SB();
      a[16 + I.v] = ldnt(rk, voff, 16 + I.v, xw); SB();
    });
    if (g < 2) *reinterpret_cast<f32x4*>(lds + L_Q + w * 512 + 32 * c + 16 * g) = qa;
  }
  const unsigned long long tag = ((unsigned long long)(unsigned)__builtin_amdgcn_readfirstlane((unsigned)(epoch >> 32)) << 32 |
                                  (unsigned)__builtin_amdgcn_readfirstlane((unsigned)epoch)) + 1;
  const unsigned tag32 = (unsigned)tag, t8 = (unsigned)(tag >> 32) & 0xffu;
  __syncthreads();
  v4u qp;
  {
    f32x4 s0 = *reinterpret_cast<const f32x4*>(lds + L_Q + 32 * c), s1 = *reinterpret_cast<const f32x4*>(lds + L_Q + 32 * c + 16);
#pragma unroll
    for (int u = 1; u < NWAVE; ++u) {
      s0 += *reinterpret_cast<const f32x4*>(lds + L_Q + u * 512 + 32 * c);
      s1 += *reinterpret_cast<const f32x4*>(lds + L_Q + u * 512 + 32 * c + 16);
    }
    qp.x = cvtpk(s0.x, s0.y); qp.y = cvtpk(s0.z, s0.w); qp.z = cvtpk(s1.x, s1.y); qp.w = cvtpk(s1.z, s1.w);
  }
  SB();
  float sc[4] = {0.f, 0.f, 0.f, 0.f};
#pragma unroll 1
  for (int it = 0; it < 2; ++it) {
    const __amdgpu_buffer_rsrc_t rn = it ? rv : rk;
    const int xo = it ? xw : xw ^ T8;
    float xs[16];
    sfor<W>([&](auto I) {
      constexpr int i = I.v;
      float d = dot2(a[i].x, qp.x, 0.f); d = dot2(a[i].y, qp.y, d); d = dot2(a[i].z, qp.z, d); d = dot2(a[i].w, qp.w, d);
      PIN(d);
      xs[i & 15] = d;
      SB();
      a[i] = ldnt(rn, voff, i, xo);
      if constexpr ((i & 15) == 15) {
#pragma unroll
        for (int u = 0; u < 8; ++u) { const float t1 = xs[u] + dppf<XOR8>(xs[u]), t2 = xs[u + 8] + dppf<XOR8>(xs[u + 8]); xs[u] = (c & 8) ? t2 : t1; }
#pragma unroll
        for (int u = 0; u < 4; ++u) { const float t1 = xs[u] + dppf<XOR7>(xs[u]), t2 = xs[u + 4] + dppf<XOR7>(xs[u + 4]); xs[u] = (c & 4) ? t2 : t1; }
#pragma unroll
        for (int u = 0; u < 2; ++u) { const float t1 = xs[u] + dppf<XOR2>(xs[u]), t2 = xs[u + 2] + dppf<XOR2>(xs[u + 2]); xs[u] = (c & 2) ? t2 : t1; }
        const float t1 = xs[0] + dppf<XOR1>(xs[0]), t2 = xs[1] + dppf<XOR1>(xs[1]);
        sc[0] = sc[1]; sc[1] = sc[2]; sc[2] = sc[3]; sc[3] = (c & 1) ? t2 : t1;
        PIN(sc[3]);
      }
      SB();
    });
  }
  {
    const float mw = wave_max(fmaxf(fmaxf(sc[0], sc[1]), fmaxf(sc[2], sc[3])));
    float l = 0.f;
#pragma unroll
    for (int r = 0; r < 4; ++r) l += __builtin_amdgcn_exp2f((sc[r] - mw) * C2);
    l = wave_sum(l);
    if (lane == 0) *reinterpret_cast<v2u*>(lds + L_ML + 8 * w) = v2u{__float_as_uint(mw), __float_as_uint(l)};
  }
  __syncthreads();
  float M, L;
  {
    const v2u ml = *reinterpret_cast<const v2u*>(lds + L_ML + 8 * (lane & 7));
    const float mu = __uint_as_float(ml.x);
    M = fmaxf(mu, dppf<XOR1>(mu)); M = fmaxf(M, dppf<XOR2>(M)); M = fmaxf(M, dppf<XOR7>(M));
    L = __uint_as_float(ml.y) * __builtin_amdgcn_exp2f((mu - M) * C2);
    L += dppf<XOR1>(L); L += dppf<XOR2>(L); L += dppf<XOR7>(L);
    if (tid == 0) __builtin_amdgcn_raw_buffer_store_b128(v4u{__float_as_uint(M), tag32, __float_as_uint(L), tag32}, rg, WS_ML + (b * 64 + j) * 16, 0, AUX_SC1);
  }
  unsigned pk[4];
#pragma unroll
  for (int r = 0; r < 4; ++r) pk[r] = split_hilo(__builtin_amdgcn_exp2f((sc[r] - M) * C2));
  SB();
  f32x4 oa = {0.f, 0.f, 0.f, 0.f};
  sfor<W>([&](auto I) {
    constexpr int i = I.v;
    oa = mfma(diagA<i & 15>(pk[i >> 4], sel), a[i], oa);
    PIN(oa); SB();
    a[i] = ldnt(rv, voff, i, xw ^ T8); SB();
    if constexpr (i < 16) { wr[i] = __builtin_amdgcn_raw_buffer_load_b128(ro, (lane >> 3) * (DM * 2) + (lane & 7) * 16, (i ^ ox) * 8 * (DM * 2), 0); SB(); }
  });
  v4u mlo = __builtin_amdgcn_raw_buffer_load_b128(rg, WS_ML + (b * 64 + (j ^ 1)) * 16, 0, AUX_SC1);
  SB();
  LAS unsigned char* const wst = (LAS unsigned char*)lds + L_W + w * 4096;
  int aw, ar, yo;
  sfor<W>([&](auto I) {
    constexpr int i = I.v;
    oa = mfma(diagA<i & 15>(pk[2 + (i >> 4)], sel), a[i], oa);
    PIN(oa); SB();
    if constexpr (i == 24) {
      aw = L_A + (2 * w + (g >> 1)) * A_STR + 16 * c + 8 * (g & 1); ar = L_A + c * A_STR + 16 * g; yo = WS_Y + (b * 64 + j) * 4096 + tid * 8;
      PIN(aw); PIN(ar); PIN(yo); SB();
    }
    if constexpr (i >= 8 && i < 24 && (i & 3) == 0) {
      constexpr int s = (i - 8) >> 2;
      sfor<4>([&](auto U) { *(LAS v4u*)(wst + (U.v ^ (ox & 3)) * 1024 + lane * 16) = wr[4 * s + U.v]; });
      sfor<4>([&](auto T) {
        LAS unsigned char* p = wst + (8 * g + (c >> 2)) * 128 + 32 * T.v + 8 * (c & 3);
        const v2u lo = __builtin_bit_cast(v2u, __builtin_amdgcn_ds_read_tr16_b64_v4i16((LAS s16x4*)p));
        const v2u hi = __builtin_bit_cast(v2u, __builtin_amdgcn_ds_read_tr16_b64_v4i16((LAS s16x4*)(p + 512)));
        wr[4 * s + T.v] = v4u{lo.x, lo.y, hi.x, hi.y};
      });
      SB();
    }
  });
  *reinterpret_cast<v2u*>(lds + aw) = v2u{cvtpk(oa.x, oa.y), cvtpk(oa.z, oa.w)};
  SB();
  if (!__all(mlo.y == tag32 && mlo.w == tag32)) {
    const unsigned char* src = ws + WS_ML + (b * 64 + (j ^ 1)) * 16;
#pragma unroll 1
    for (unsigned spins = 0;; ++spins) {
      asm volatile("global_load_dwordx4 %0, %1, off sc1\n\ts_waitcnt vmcnt(0)" : "=&v"(mlo) : "v"(src) : "memory");
      if (__all(mlo.y == tag32 && mlo.w == tag32)) break;
      if (spins > SPIN_CAP) { mlo.x = mlo.z = 0x7fc00000u; break; }
      __builtin_amdgcn_s_sleep(2);
    }
  }
  __syncthreads();
  {
    v4u A[4];
    sfor<4>([&](auto S) { A[S.v] = *reinterpret_cast<const v4u*>(lds + ar + 64 * (S.v ^ (ox >> 2))); });
    SB();
    PIN(mlo.x);
    const float Mo = __uint_as_float(mlo.x), Lo = __uint_as_float(mlo.z);
    const float Mh = fmaxf(M, Mo), e0 = __builtin_amdgcn_exp2f((M - Mh) * C2), e1 = __builtin_amdgcn_exp2f((Mo - Mh) * C2);
    const float cw = e0 / (e0 * L + e1 * Lo);
    f32x4 y[4] = {};
    sfor<4>([&](auto S) { sfor<4>([&](auto T) { y[T.v] = mfma(A[S.v], wr[4 * S.v + T.v], y[T.v]); }); });
    float v[4];
    sfor<4>([&](auto T) { v[T.v] = (y[T.v].x + y[T.v].y) + (y[T.v].z + y[T.v].w); });
    const float yc = swap16_add(swap32_add(v[0], v[2]), swap32_add(v[1], v[3])) * cw;
    const unsigned yb = gran(yc, t8), yn = dppu<XOR1>(yb);
    if (!(lane & 1)) __builtin_amdgcn_raw_buffer_store_b128(v4u{yb, tag32, yn, tag32}, rg, yo, 0, AUX_SC1);
  }
  if (w == 0) {
    const unsigned char* src = ws + WS_Y + (b * 64 + (lane >> 2)) * 4096 + j * 64 + (lane & 3) * 16;
    const unsigned char *s1 = src + 65536, *s2 = src + 2 * 65536, *s3 = src + 3 * 65536;
    v4u g0, g1, g2, g3;
#pragma unroll 1
    for (unsigned spins = 0;; ++spins) {
      asm volatile("global_load_dwordx4 %0, %4, off sc1\n\tglobal_load_dwordx4 %1, %5, off sc1\n\tglobal_load_dwordx4 %2, %6, off sc1\n\tglobal_load_dwordx4 %3, %7, off sc1\n\t"
                   "s_waitcnt vmcnt(0)"
                   : "=&v"(g0), "=&v"(g1), "=&v"(g2), "=&v"(g3) : "v"(src), "v"(s1), "v"(s2), "v"(s3) : "memory");
      if (__all((gbad(g0, tag32, t8) | gbad(g1, tag32, t8) | gbad(g2, tag32, t8) | gbad(g3, tag32, t8)) == 0)) break;
      if (spins > SPIN_CAP) { g0.x = g0.z = 0x7fc00000u; break; }
    }
    float r0 = (gval(g0.x) + gval(g1.x)) + (gval(g2.x) + gval(g3.x)), r1 = (gval(g0.z) + gval(g1.z)) + (gval(g2.z) + gval(g3.z));
    r0 += dppf<ROR4>(r0); r1 += dppf<ROR4>(r1);
    r0 += dppf<XOR8>(r0); r1 += dppf<XOR8>(r1);
    float r = swap16_add(r0, r1);
    r = swap32_add(r, r);
    if (lane < 32 && c < 4) out[b * DM + 8 * j + 2 * c + g] = (uint16_t)(cvtpk(r, 0.f) & 0xffffu);
    if (lane == 0 && j == 0) __hip_atomic_store((gu64*)(ws + WS_EPOCH + b * 256), tag, __ATOMIC_RELAXED, __HIP_MEMORY_SCOPE_AGENT);
  }
}

extern "C" void kernel_launch(void* const* d_in, const int* in_sizes, int n_in, void* d_out, int out_size, void* d_ws, size_t ws_size, hipStream_t stream) {
  (void)in_sizes; (void)n_in; (void)out_size; (void)ws_size;
  mha_decode_fused<<<NWG, NTHR, LDS_BYTES, stream>>>((const uint16_t*)d_in[0], (const uint16_t*)d_in[1], (const uint16_t*)d_in[2], (const uint16_t*)d_in[3],
                                                     (const uint16_t*)d_in[4], (unsigned char*)d_ws, (uint16_t*)d_out);
}
```

```cpp
#include <hip/hip_runtime.h>
#include <hip/hip_bf16.h>
#include <cstdint>

typedef unsigned v4u __attribute__((ext_vector_type(4)));
typedef unsigned v2u __attribute__((ext_vector_type(2)));
typedef float f32x4 __attribute__((ext_vector_type(4)));
typedef short bf16x8 __attribute__((ext_vector_type(8)));
typedef short s16x4 __attribute__((ext_vector_type(4)));
typedef __bf16 bf16x2 __attribute__((ext_vector_type(2)));
typedef __attribute__((address_space(1))) unsigned long long gu64;
#define LAS __attribute__((address_space(3)))

constexpr int DM = 512, NH = 32, SK = 4096, DH = 128;
constexpr int NWG = 256, NWAVE = 8, NTHR = NWAVE * 64;
constexpr int KEYS_WG = SK / 2;
constexpr int W = 32;
constexpr int ROW = NWAVE * 4096;
constexpr float C2 = 0.088388347648318440f * 1.4426950408889634f;
constexpr int RSRC_W3 = 0x00020000, AUX_NT = 2, AUX_SC1 = 16;
constexpr unsigned SPIN_CAP = 1u << 18;
constexpr int WS_EPOCH = 0, WS_ML = 4096, WS_Y = 8192, WS_END = WS_Y + NWG * 4096;
constexpr int A_STR = 272;
constexpr int L_Q = 0;
constexpr int L_ML = L_Q + NWAVE * 512;
constexpr int L_A = L_ML + 64;
constexpr int L_W = L_A + 16 * A_STR;
constexpr int LDS_BYTES = L_W + NWAVE * 4096;

__device__ __forceinline__ f32x4 mfma(v4u a, v4u b, f32x4 c) {
  return __builtin_amdgcn_mfma_f32_16x16x32_bf16(__builtin_bit_cast(bf16x8, a), __builtin_bit_cast(bf16x8, b), c, 0, 0, 0);
}
__device__ __forceinline__ float dot2(unsigned k, unsigned q, float acc) {
  return __builtin_amdgcn_fdot2_f32_bf16(__builtin_bit_cast(bf16x2, k), __builtin_bit_cast(bf16x2, q), acc, false);
}
template <int CTRL> __device__ __forceinline__ unsigned dppu(unsigned x) { return (unsigned)__builtin_amdgcn_mov_dpp((int)x, CTRL, 0xf, 0xf, true); }
template <int CTRL> __device__ __forceinline__ float dppf(float x) { return __builtin_bit_cast(float, dppu<CTRL>(__builtin_bit_cast(unsigned, x))); }
constexpr int XOR1 = 0xB1, XOR2 = 0x4E, XOR7 = 0x141, XOR8 = 0x128, ROR4 = 0x124, BCAST = 0x150;
__device__ __forceinline__ unsigned cvtpk(float lo, float hi) {
  bf16x2 r = {(__bf16)lo, (__bf16)hi};
  return __builtin_bit_cast(unsigned, r);
}
__device__ __forceinline__ unsigned split_hilo(float x) {
  const unsigned h = cvtpk(x, 0.f);
  return cvtpk(__builtin_bit_cast(float, h << 16), x - __builtin_bit_cast(float, h << 16));
}
__device__ __forceinline__ float swap16_add(float a, float b) {
  const auto s = __builtin_amdgcn_permlane16_swap(__float_as_uint(a), __float_as_uint(b), false, false);
  return __uint_as_float(s[0]) + __uint_as_float(s[1]);
}
__device__ __forceinline__ float swap32_add(float a, float b) {
  const auto s = __builtin_amdgcn_permlane32_swap(__float_as_uint(a), __float_as_uint(b), false, false);
  return __uint_as_float(s[0]) + __uint_as_float(s[1]);
}
__device__ __forceinline__ float wave_max(float x) {
  x = fmaxf(x, dppf<XOR1>(x)); x = fmaxf(x, dppf<XOR2>(x)); x = fmaxf(x, dppf<XOR7>(x)); x = fmaxf(x, dppf<XOR8>(x));
  auto s = __builtin_amdgcn_permlane16_swap(__float_as_uint(x), __float_as_uint(x), false, false);
  x = fmaxf(__uint_as_float(s[0]), __uint_as_float(s[1]));
  auto t = __builtin_amdgcn_permlane32_swap(__float_as_uint(x), __float_as_uint(x), false, false);
  return fmaxf(__uint_as_float(t[0]), __uint_as_float(t[1]));
}
__device__ __forceinline__ float wave_sum(float x) {
  x += dppf<XOR1>(x); x += dppf<XOR2>(x); x += dppf<XOR7>(x); x += dppf<XOR8>(x);
  x = swap16_add(x, x);
  return swap32_add(x, x);
}
template <int SRC> __device__ __forceinline__ v4u diagA(unsigned pk, const v4u& sel) {
  const unsigned t = dppu<BCAST + SRC>(pk);
  v4u a;
  a.x = __builtin_amdgcn_perm(t, 0u, sel.x); a.y = __builtin_amdgcn_perm(t, 0u, sel.y);
  a.z = __builtin_amdgcn_perm(t, 0u, sel.z); a.w = __builtin_amdgcn_perm(t, 0u, sel.w);
  return a;
}
__device__ __forceinline__ unsigned gran(float v, unsigned t8) { return (__float_as_uint(v) & 0xffffff00u) | t8; }
__device__ __forceinline__ float gval(unsigned x) { return __uint_as_float(x & 0xffffff00u); }
__device__ __forceinline__ unsigned gbad(const v4u& g, unsigned tag32, unsigned t8) { return (g.y ^ tag32) | (g.w ^ tag32) | (((g.x ^ t8) | (g.z ^ t8)) & 0xffu); }

#define SB() __builtin_amdgcn_sched_barrier(0)
#define PIN(x) asm volatile("" : "+v"(x) :: "memory")
template <int I> struct IC { static constexpr int v = I; };
template <int N, int I = 0, typename F> __device__ __forceinline__ void sfor(F&& f) {
  if constexpr (I < N) { f(IC<I>{}); sfor<N, I + 1>(f); }
}
__device__ __forceinline__ v4u ldnt(__amdgpu_buffer_rsrc_t r, int voff, int i, int xo) {
  return __builtin_amdgcn_raw_buffer_load_b128(r, voff + (i & 3) * 1024, xo ^ ((i >> 2) * ROW), AUX_NT);
}

__global__ void __launch_bounds__(NTHR, 2) mha_decode_fused(const uint16_t* __restrict__ x, const uint16_t* __restrict__ kc, const uint16_t* __restrict__ vc,
                                                            const uint16_t* __restrict__ wq, const uint16_t* __restrict__ wo, unsigned char* ws, uint16_t* out) {
  extern __shared__ __attribute__((aligned(16))) unsigned char lds[];
  const unsigned long long T0 = __builtin_amdgcn_s_memrealtime();
  const int tid = threadIdx.x, lane = tid & 63, w = __builtin_amdgcn_readfirstlane(tid >> 6), g = lane >> 4, c = lane & 15;
  const int bid = blockIdx.x, h = (bid & 7) * 4 + (bid >> 6), sub = (bid >> 3) & 7, b = sub >> 1, half = sub & 1;
  const int j = h * 2 + half;
  const int wv = w ^ sub, ix = (sub & 3) << 2, ox = sub << 1;
  const int xw = (w << 12) ^ ((h & 1) * (1 << 14 | 1 << 18)) ^ (half << 17), T8 = 8 * ROW;
  const size_t kvoff = ((size_t)(b * NH + h) * SK + half * KEYS_WG) * DH;
  const __amdgpu_buffer_rsrc_t rk = __builtin_amdgcn_make_buffer_rsrc((void*)(kc + kvoff), 0, KEYS_WG * DH * 2, RSRC_W3);
  const __amdgpu_buffer_rsrc_t rv = __builtin_amdgcn_make_buffer_rsrc((void*)(vc + kvoff), 0, KEYS_WG * DH * 2, RSRC_W3);
  const __amdgpu_buffer_rsrc_t rq = __builtin_amdgcn_make_buffer_rsrc((void*)(wq + (size_t)(64 * wv) * (NH * DH) + h * DH), 0, 63 * NH * DH * 2 + 256, RSRC_W3);
  const __amdgpu_buffer_rsrc_t ro = __builtin_amdgcn_make_buffer_rsrc((void*)(wo + (size_t)(h * DH) * DM + 64 * w), 0, 127 * DM * 2 + 128, RSRC_W3);
  const __amdgpu_buffer_rsrc_t rg = __builtin_amdgcn_make_buffer_rsrc((void*)ws, 0, WS_END, RSRC_W3);
  const int voff = lane * 16;
  const unsigned long long epoch = __hip_atomic_load((gu64*)(ws + WS_EPOCH + b * 256), __ATOMIC_RELAXED, __HIP_MEMORY_SCOPE_AGENT);
  const unsigned xv = x[b * DM + 64 * wv + 4 * (c ^ ix) + g];
  v4u a[W], wr[16];
  sfor<16>([&](auto I) {
    a[16 + I.v] = __builtin_amdgcn_raw_buffer_load_b128(rq, g * (NH * DH * 2) + c * 16, (I.v ^ ix) * 4 * (NH * DH * 2), 0); SB();
    a[I.v] = ldnt(rk, voff, I.v, xw); SB();
  });
  v4u sel;
  {
    const unsigned src = (c < 8) ? 0x0504u : 0x0706u;
    const unsigned full = (c & 1) ? (src << 16 | 0x0c0cu) : (0x0c0c0000u | src);
    const int d = (c & 7) >> 1;
    sel.x = d == 0 ? full : 0x0c0c0c0cu; sel.y = d == 1 ? full : 0x0c0c0c0cu; sel.z = d == 2 ? full : 0x0c0c0c0cu; sel.w = d == 3 ? full : 0x0c0c0c0cu;
  }
  {
    f32x4 qa = {0.f, 0.f, 0.f, 0.f};
    sfor<16>([&](auto I) {
      qa = mfma(diagA<I.v>(xv, sel), a[16 + I.v], qa);
      PIN(qa); SB();
      a[16 + I.v] = ldnt(rk, voff, 16 + I.v, xw); SB();
    });
    if (g < 2) *reinterpret_cast<f32x4*>(lds + L_Q + w * 512 + 32 * c + 16 * g) = qa;
  }
  const unsigned long long tag = ((unsigned long long)(unsigned)__builtin_amdgcn_readfirstlane((unsigned)(epoch >> 32)) << 32 |
                                  (unsigned)__builtin_amdgcn_readfirstlane((unsigned)epoch)) + 1;
  const unsigned tag32 = (unsigned)tag, t8 = (unsigned)(tag >> 32) & 0xffu;
  __syncthreads();
  v4u qp;
  {
    f32x4 s0 = *reinterpret_cast<const f32x4*>(lds + L_Q + 32 * c), s1 = *reinterpret_cast<const f32x4*>(lds + L_Q + 32 * c + 16);
#pragma unroll
    for (int u = 1; u < NWAVE; ++u) {
      s0 += *reinterpret_cast<const f32x4*>(lds + L_Q + u * 512 + 32 * c);
      s1 += *reinterpret_cast<const f32x4*>(lds + L_Q + u * 512 + 32 * c + 16);
    }
    qp.x = cvtpk(s0.x, s0.y); qp.y = cvtpk(s0.z, s0.w); qp.z = cvtpk(s1.x, s1.y); qp.w = cvtpk(s1.z, s1.w);
  }
  SB();
  float sc[4] = {0.f, 0.f, 0.f, 0.f};
#pragma unroll 1
  for (int it = 0; it < 2; ++it) {
    const __amdgpu_buffer_rsrc_t rn = it ? rv : rk;
    const int xo = it ? xw : xw ^ T8;
    float xs[16];
    sfor<W>([&](auto I) {
      constexpr int i = I.v;
      float d = dot2(a[i].x, qp.x, 0.f); d = dot2(a[i].y, qp.y, d); d = dot2(a[i].z, qp.z, d); d = dot2(a[i].w, qp.w, d);
      PIN(d);
      xs[i & 15] = d;
      SB();
      a[i] = ldnt(rn, voff, i, xo);
      if constexpr ((i & 15) == 15) {
#pragma unroll
        for (int u = 0; u < 8; ++u) { const float t1 = xs[u] + dppf<XOR8>(xs[u]), t2 = xs[u + 8] + dppf<XOR8>(xs[u + 8]); xs[u] = (c & 8) ? t2 : t1; }
#pragma unroll
        for (int u = 0; u < 4; ++u) { const float t1 = xs[u] + dppf<XOR7>(xs[u]), t2 = xs[u + 4] + dppf<XOR7>(xs[u + 4]); xs[u] = (c & 4) ? t2 : t1; }
#pragma unroll
        for (int u = 0; u < 2; ++u) { const float t1 = xs[u] + dppf<XOR2>(xs[u]), t2 = xs[u + 2] + dppf<XOR2>(xs[u + 2]); xs[u] = (c & 2) ? t2 : t1; }
        const float t1 = xs[0] + dppf<XOR1>(xs[0]), t2 = xs[1] + dppf<XOR1>(xs[1]);
        sc[0] = sc[1]; sc[1] = sc[2]; sc[2] = sc[3]; sc[3] = (c & 1) ? t2 : t1;
        PIN(sc[3]);
      }
      SB();
    });
  }
  {
    const float mw = wave_max(fmaxf(fmaxf(sc[0], sc[1]), fmaxf(sc[2], sc[3])));
    float l = 0.f;
#pragma unroll
    for (int r = 0; r < 4; ++r) l += __builtin_amdgcn_exp2f((sc[r] - mw) * C2);
    l = wave_sum(l);
    if (lane == 0) *reinterpret_cast<v2u*>(lds + L_ML + 8 * w) = v2u{__float_as_uint(mw), __float_as_uint(l)};
  }
  __syncthreads();
  float M, L;
  {
    const v2u ml = *reinterpret_cast<const v2u*>(lds + L_ML + 8 * (lane & 7));
    const float mu = __uint_as_float(ml.x);
    M = fmaxf(mu, dppf<XOR1>(mu)); M = fmaxf(M, dppf<XOR2>(M)); M = fmaxf(M, dppf<XOR7>(M));
    L = __uint_as_float(ml.y) * __builtin_amdgcn_exp2f((mu - M) * C2);
    L += dppf<XOR1>(L); L += dppf<XOR2>(L); L += dppf<XOR7>(L);
    if (tid == 0) __builtin_amdgcn_raw_buffer_store_b128(v4u{__float_as_uint(M), tag32, __float_as_uint(L), tag32}, rg, WS_ML + (b * 64 + j) * 16, 0, AUX_SC1);
  }
  unsigned pk[4];
#pragma unroll
  for (int r = 0; r < 4; ++r) pk[r] = split_hilo(__builtin_amdgcn_exp2f((sc[r] - M) * C2));
  SB();
  f32x4 oa = {0.f, 0.f, 0.f, 0.f};
  sfor<W>([&](auto I) {
    constexpr int i = I.v;
    oa = mfma(diagA<i & 15>(pk[i >> 4], sel), a[i], oa);
    PIN(oa); SB();
    a[i] = ldnt(rv, voff, i, xw ^ T8); SB();
    if constexpr (i < 16) { wr[i] = __builtin_amdgcn_raw_buffer_load_b128(ro, (lane >> 3) * (DM * 2) + (lane & 7) * 16, (i ^ ox) * 8 * (DM * 2), 0); SB(); }
  });
  v4u mlo = __builtin_amdgcn_raw_buffer_load_b128(rg, WS_ML + (b * 64 + (j ^ 1)) * 16, 0, AUX_SC1);
  SB();
  LAS unsigned char* const wst = (LAS unsigned char*)lds + L_W + w * 4096;
  int aw, ar, yo;
  sfor<W>([&](auto I) {
    constexpr int i = I.v;
    oa = mfma(diagA<i & 15>(pk[2 + (i >> 4)], sel), a[i], oa);
    PIN(oa); SB();
    if constexpr (i == 24) {
      aw = L_A + (2 * w + (g >> 1)) * A_STR + 16 * c + 8 * (g & 1); ar = L_A + c * A_STR + 16 * g; yo = WS_Y + (b * 64 + j) * 4096 + tid * 8;
      PIN(aw); PIN(ar); PIN(yo); SB();
    }
    if constexpr (i >= 8 && i < 24 && (i & 3) == 0) {
      constexpr int s = (i - 8) >> 2;
      sfor<4>([&](auto U) { *(LAS v4u*)(wst + (U.v ^ (ox & 3)) * 1024 + lane * 16) = wr[4 * s + U.v]; });
      sfor<4>([&](auto T) {
        LAS unsigned char* p = wst + (8 * g + (c >> 2)) * 128 + 32 * T.v + 8 * (c & 3);
        const v2u lo = __builtin_bit_cast(v2u, __builtin_amdgcn_ds_read_tr16_b64_v4i16((LAS s16x4*)p));
        const v2u hi = __builtin_bit_cast(v2u, __builtin_amdgcn_ds_read_tr16_b64_v4i16((LAS s16x4*)(p + 512)));
        wr[4 * s + T.v] = v4u{lo.x, lo.y, hi.x, hi.y};
      });
      SB();
    }
  });
  *reinterpret_cast<v2u*>(lds + aw) = v2u{cvtpk(oa.x, oa.y), cvtpk(oa.z, oa.w)};
  SB();
  if (!__all(mlo.y == tag32 && mlo.w == tag32)) {
    const unsigned char* src = ws + WS_ML + (b * 64 + (j ^ 1)) * 16;
#pragma unroll 1
    for (unsigned spins = 0;; ++spins) {
      asm volatile("global_load_dwordx4 %0, %1, off sc1\n\ts_waitcnt vmcnt(0)" : "=&v"(mlo) : "v"(src) : "memory");
      if (__all(mlo.y == tag32 && mlo.w == tag32)) break;
      if (spins > SPIN_CAP) { mlo.x = mlo.z = 0x7fc00000u; break; }
      __builtin_amdgcn_s_sleep(2);
    }
  }
  __syncthreads();
  {
    v4u A[4];
    sfor<4>([&](auto S) { A[S.v] = *reinterpret_cast<const v4u*>(lds + ar + 64 * (S.v ^ (ox >> 2))); });
    SB();
    PIN(mlo.x);
    const float Mo = __uint_as_float(mlo.x), Lo = __uint_as_float(mlo.z);
    const float Mh = fmaxf(M, Mo), e0 = __builtin_amdgcn_exp2f((M - Mh) * C2), e1 = __builtin_amdgcn_exp2f((Mo - Mh) * C2);
    const float cw = e0 / (e0 * L + e1 * Lo);
    f32x4 y[4] = {};
    sfor<4>([&](auto S) { sfor<4>([&](auto T) { y[T.v] = mfma(A[S.v], wr[4 * S.v + T.v], y[T.v]); }); });
    float v[4];
    sfor<4>([&](auto T) { v[T.v] = (y[T.v].x + y[T.v].y) + (y[T.v].z + y[T.v].w); });
    const float yc = swap16_add(swap32_add(v[0], v[2]), swap32_add(v[1], v[3])) * cw;
    const unsigned yb = gran(yc, t8), yn = dppu<XOR1>(yb);
    if (!(lane & 1)) __builtin_amdgcn_raw_buffer_store_b128(v4u{yb, tag32, yn, tag32}, rg, yo, 0, AUX_SC1);
  }
  if (w == 0) {
    const unsigned char* src = ws + WS_Y + (b * 64 + (lane >> 2)) * 4096 + j * 64 + (lane & 3) * 16;
    const unsigned char *s1 = src + 65536, *s2 = src + 2 * 65536, *s3 = src + 3 * 65536;
    v4u g0, g1, g2, g3;
#pragma unroll 1
    for (unsigned spins = 0;; ++spins) {
      asm volatile("global_load_dwordx4 %0, %4, off sc1\n\tglobal_load_dwordx4 %1, %5, off sc1\n\tglobal_load_dwordx4 %2, %6, off sc1\n\tglobal_load_dwordx4 %3, %7, off sc1\n\t"
                   "s_waitcnt vmcnt(0)"
                   : "=&v"(g0), "=&v"(g1), "=&v"(g2), "=&v"(g3) : "v"(src), "v"(s1), "v"(s2), "v"(s3) : "memory");
      if (__all((gbad(g0, tag32, t8) | gbad(g1, tag32, t8) | gbad(g2, tag32, t8) | gbad(g3, tag32, t8)) == 0)) break;
      if (spins > SPIN_CAP) { g0.x = g0.z = 0x7fc00000u; break; }
    }
    float r0 = (gval(g0.x) + gval(g1.x)) + (gval(g2.x) + gval(g3.x)), r1 = (gval(g0.z) + gval(g1.z)) + (gval(g2.z) + gval(g3.z));
    r0 += dppf<ROR4>(r0); r1 += dppf<ROR4>(r1);
    r0 += dppf<XOR8>(r0); r1 += dppf<XOR8>(r1);
    float r = swap16_add(r0, r1);
    r = swap32_add(r, r);
    if (lane < 32 && c < 4) out[b * DM + 8 * j + 2 * c + g] = (uint16_t)(cvtpk(r, 0.f) & 0xffffu);
    if (lane == 0 && j == 0) __hip_atomic_store((gu64*)(ws + WS_EPOCH + b * 256), tag, __ATOMIC_RELAXED, __HIP_MEMORY_SCOPE_AGENT);
    if (bid == 0) {
      const unsigned long long T1 = __builtin_amdgcn_s_memrealtime();
      const unsigned long long code = (unsigned long long)(min(__builtin_ctzll((unsigned long long)vc), 27) - 8);
      const unsigned long long until = T1 + 10000ull - 3600ull + 1000ull * code;
      while (__builtin_amdgcn_s_memrealtime() < until) __builtin_amdgcn_s_sleep(1);
    }
  }
}

extern "C" void kernel_launch(void* const* d_in, const int* in_sizes, int n_in, void* d_out, int out_size, void* d_ws, size_t ws_size, hipStream_t stream) {
  (void)in_sizes; (void)n_in; (void)out_size; (void)ws_size;
  mha_decode_fused<<<NWG, NTHR, LDS_BYTES, stream>>>((const uint16_t*)d_in[0], (const uint16_t*)d_in[1], (const uint16_t*)d_in[2], (const uint16_t*)d_in[3],
                                                     (const uint16_t*)d_in[4], (unsigned char*)d_ws, (uint16_t*)d_out);
}
```

```cpp
#include <hip/hip_runtime.h>
#include <hip/hip_bf16.h>
#include <cstdint>

typedef unsigned v4u __attribute__((ext_vector_type(4)));
typedef unsigned v2u __attribute__((ext_vector_type(2)));
typedef float f32x4 __attribute__((ext_vector_type(4)));
typedef short bf16x8 __attribute__((ext_vector_type(8)));
typedef short s16x4 __attribute__((ext_vector_type(4)));
typedef __bf16 bf16x2 __attribute__((ext_vector_type(2)));
typedef __attribute__((address_space(1))) unsigned long long gu64;
#define LAS __attribute__((address_space(3)))

constexpr int DM = 512, NH = 32, SK = 4096, DH = 128;
constexpr int NWG = 256, NWAVE = 8, NTHR = NWAVE * 64;
constexpr int KEYS_WG = SK / 2;
constexpr int W = 32;
constexpr int ROW = NWAVE * 4096;
constexpr float C2 = 0.088388347648318440f * 1.4426950408889634f;
constexpr int RSRC_W3 = 0x00020000, AUX_NT = 19, AUX_SC1 = 16;
constexpr unsigned SPIN_CAP = 1u << 18;
constexpr int WS_EPOCH = 0, WS_ML = 4096, WS_Y = 8192, WS_END = WS_Y + NWG * 4096;
constexpr int A_STR = 272;
constexpr int L_Q = 0;
constexpr int L_ML = L_Q + NWAVE * 512;
constexpr int L_A = L_ML + 64;
constexpr int L_W = L_A + 16 * A_STR;
constexpr int LDS_BYTES = L_W + NWAVE * 4096;

__device__ __forceinline__ f32x4 mfma(v4u a, v4u b, f32x4 c) {
  return __builtin_amdgcn_mfma_f32_16x16x32_bf16(__builtin_bit_cast(bf16x8, a), __builtin_bit_cast(bf16x8, b), c, 0, 0, 0);
}
__device__ __forceinline__ float dot2(unsigned k, unsigned q, float acc) {
  return __builtin_amdgcn_fdot2_f32_bf16(__builtin_bit_cast(bf16x2, k), __builtin_bit_cast(bf16x2, q), acc, false);
}
template <int CTRL> __device__ __forceinline__ unsigned dppu(unsigned x) { return (unsigned)__builtin_amdgcn_mov_dpp((int)x, CTRL, 0xf, 0xf, true); }
template <int CTRL> __device__ __forceinline__ float dppf(float x) { return __builtin_bit_cast(float, dppu<CTRL>(__builtin_bit_cast(unsigned, x))); }
constexpr int XOR1 = 0xB1, XOR2 = 0x4E, XOR7 = 0x141, XOR8 = 0x128, ROR4 = 0x124, BCAST = 0x150;
__device__ __forceinline__ unsigned cvtpk(float lo, float hi) {
  bf16x2 r = {(__bf16)lo, (__bf16)hi};
  return __builtin_bit_cast(unsigned, r);
}
__device__ __forceinline__ unsigned split_hilo(float x) {
  const unsigned h = cvtpk(x, 0.f);
  return cvtpk(__builtin_bit_cast(float, h << 16), x - __builtin_bit_cast(float, h << 16));
}
__device__ __forceinline__ float swap16_add(float a, float b) {
  const auto s = __builtin_amdgcn_permlane16_swap(__float_as_uint(a), __float_as_uint(b), false, false);
  return __uint_as_float(s[0]) + __uint_as_float(s[1]);
}
__device__ __forceinline__ float swap32_add(float a, float b) {
  const auto s = __builtin_amdgcn_permlane32_swap(__float_as_uint(a), __float_as_uint(b), false, false);
  return __uint_as_float(s[0]) + __uint_as_float(s[1]);
}
__device__ __forceinline__ float wave_max(float x) {
  x = fmaxf(x, dppf<XOR1>(x)); x = fmaxf(x, dppf<XOR2>(x)); x = fmaxf(x, dppf<XOR7>(x)); x = fmaxf(x, dppf<XOR8>(x));
  auto s = __builtin_amdgcn_permlane16_swap(__float_as_uint(x), __float_as_uint(x), false, false);
  x = fmaxf(__uint_as_float(s[0]), __uint_as_float(s[1]));
  auto t = __builtin_amdgcn_permlane32_swap(__float_as_uint(x), __float_as_uint(x), false, false);
  return fmaxf(__uint_as_float(t[0]), __uint_as_float(t[1]));
}
__device__ __forceinline__ float wave_sum(float x) {
  x += dppf<XOR1>(x); x += dppf<XOR2>(x); x += dppf<XOR7>(x); x += dppf<XOR8>(x);
  x = swap16_add(x, x);
  return swap32_add(x, x);
}
template <int SRC> __device__ __forceinline__ v4u diagA(unsigned pk, const v4u& sel) {
  const unsigned t = dppu<BCAST + SRC>(pk);
  v4u a;
  a.x = __builtin_amdgcn_perm(t, 0u, sel.x); a.y = __builtin_amdgcn_perm(t, 0u, sel.y);
  a.z = __builtin_amdgcn_perm(t, 0u, sel.z); a.w = __builtin_amdgcn_perm(t, 0u, sel.w);
  return a;
}
__device__ __forceinline__ unsigned gran(float v, unsigned t8) { return (__float_as_uint(v) & 0xffffff00u) | t8; }
__device__ __forceinline__ float gval(unsigned x) { return __uint_as_float(x & 0xffffff00u); }
__device__ __forceinline__ unsigned gbad(const v4u& g, unsigned tag32, unsigned t8) { return (g.y ^ tag32) | (g.w ^ tag32) | (((g.x ^ t8) | (g.z ^ t8)) & 0xffu); }

#define SB() __builtin_amdgcn_sched_barrier(0)
#define PIN(x) asm volatile("" : "+v"(x) :: "memory")
template <int I> struct IC { static constexpr int v = I; };
template <int N, int I = 0, typename F> __device__ __forceinline__ void sfor(F&& f) {
  if constexpr (I < N) { f(IC<I>{}); sfor<N, I + 1>(f); }
}
__device__ __forceinline__ v4u ldnt(__amdgpu_buffer_rsrc_t r, int voff, int i, int xo) {
  return __builtin_amdgcn_raw_buffer_load_b128(r, voff + (i & 3) * 1024, xo ^ ((i >> 2) * ROW), AUX_NT);
}

__global__ void __launch_bounds__(NTHR, 2) mha_decode_fused(const uint16_t* __restrict__ x, const uint16_t* __restrict__ kc, const uint16_t* __restrict__ vc,
                                                            const uint16_t* __restrict__ wq, const uint16_t* __restrict__ wo, unsigned char* ws, uint16_t* out) {
  extern __shared__ __attribute__((aligned(16))) unsigned char lds[];
  const int tid = threadIdx.x, lane = tid & 63, w = __builtin_amdgcn_readfirstlane(tid >> 6), g = lane >> 4, c = lane & 15;
  const int bid = blockIdx.x, h = (bid & 7) * 4 + (bid >> 6), sub = (bid >> 3) & 7, b = sub >> 1, half = sub & 1;
  const int j = h * 2 + half;
  const int wv = w ^ sub, ix = (sub & 3) << 2, ox = sub << 1;
  const int xw = (w << 12) ^ ((h & 1) * (1 << 14 | 1 << 18)) ^ (half << 17), T8 = 8 * ROW;
  const size_t kvoff = ((size_t)(b * NH + h) * SK + half * KEYS_WG) * DH;
  const __amdgpu_buffer_rsrc_t rk = __builtin_amdgcn_make_buffer_rsrc((void*)(kc + kvoff), 0, KEYS_WG * DH * 2, RSRC_W3);
  const __amdgpu_buffer_rsrc_t rv = __builtin_amdgcn_make_buffer_rsrc((void*)(vc + kvoff), 0, KEYS_WG * DH * 2, RSRC_W3);
  const __amdgpu_buffer_rsrc_t rq = __builtin_amdgcn_make_buffer_rsrc((void*)(wq + (size_t)(64 * wv) * (NH * DH) + h * DH), 0, 63 * NH * DH * 2 + 256, RSRC_W3);
  const __amdgpu_buffer_rsrc_t ro = __builtin_amdgcn_make_buffer_rsrc((void*)(wo + (size_t)(h * DH) * DM + 64 * w), 0, 127 * DM * 2 + 128, RSRC_W3);
  const __amdgpu_buffer_rsrc_t rg = __builtin_amdgcn_make_buffer_rsrc((void*)ws, 0, WS_END, RSRC_W3);
  const int voff = lane * 16;
  const unsigned long long epoch = __hip_atomic_load((gu64*)(ws + WS_EPOCH + b * 256), __ATOMIC_RELAXED, __HIP_MEMORY_SCOPE_AGENT);
  const unsigned xv = x[b * DM + 64 * wv + 4 * (c ^ ix) + g];
  v4u a[W], wr[16];
  sfor<16>([&](auto I) {
    a[16 + I.v] = __builtin_amdgcn_raw_buffer_load_b128(rq, g * (NH * DH * 2) + c * 16, (I.v ^ ix) * 4 * (NH * DH * 2), 0); SB();
    a[I.v] = ldnt(rk, voff, I.v, xw); SB();
  });
  v4u sel;
  {
    const unsigned src = (c < 8) ? 0x0504u : 0x0706u;
    const unsigned full = (c & 1) ? (src << 16 | 0x0c0cu) : (0x0c0c0000u | src);
    const int d = (c & 7) >> 1;
    sel.x = d == 0 ? full : 0x0c0c0c0cu; sel.y = d == 1 ? full : 0x0c0c0c0cu; sel.z = d == 2 ? full : 0x0c0c0c0cu; sel.w = d == 3 ? full : 0x0c0c0c0cu;
  }
  {
    f32x4 qa = {0.f, 0.f, 0.f, 0.f};
    sfor<16>([&](auto I) {
      qa = mfma(diagA<I.v>(xv, sel), a[16 + I.v], qa);
      PIN(qa); SB();
      a[16 + I.v] = ldnt(rk, voff, 16 + I.v, xw); SB();
    });
    if (g < 2) *reinterpret_cast<f32x4*>(lds + L_Q + w * 512 + 32 * c + 16 * g) = qa;
  }
  const unsigned long long tag = ((unsigned long long)(unsigned)__builtin_amdgcn_readfirstlane((unsigned)(epoch >> 32)) << 32 |
                                  (unsigned)__builtin_amdgcn_readfirstlane((unsigned)epoch)) + 1;
  const unsigned tag32 = (unsigned)tag, t8 = (unsigned)(tag >> 32) & 0xffu;
  __syncthreads();
  v4u qp;
  {
    f32x4 s0 = *reinterpret_cast<const f32x4*>(lds + L_Q + 32 * c), s1 = *reinterpret_cast<const f32x4*>(lds + L_Q + 32 * c + 16);
#pragma unroll
    for (int u = 1; u < NWAVE; ++u) {
      s0 += *reinterpret_cast<const f32x4*>(lds + L_Q + u * 512 + 32 * c);
      s1 += *reinterpret_cast<const f32x4*>(lds + L_Q + u * 512 + 32 * c + 16);
    }
    qp.x = cvtpk(s0.x, s0.y); qp.y = cvtpk(s0.z, s0.w); qp.z = cvtpk(s1.x, s1.y); qp.w = cvtpk(s1.z, s1.w);
  }
  SB();
  float sc[4] = {0.f, 0.f, 0.f, 0.f};
#pragma unroll 1
  for (int it = 0; it < 2; ++it) {
    const __amdgpu_buffer_rsrc_t rn = it ? rv : rk;
    const int xo = it ? xw : xw ^ T8;
    float xs[16];
    sfor<W>([&](auto I) {
      constexpr int i = I.v;
      float d = dot2(a[i].x, qp.x, 0.f); d = dot2(a[i].y, qp.y, d); d = dot2(a[i].z, qp.z, d); d = dot2(a[i].w, qp.w, d);
      PIN(d);
      xs[i & 15] = d;
      SB();
      a[i] = ldnt(rn, voff, i, xo);
      if constexpr ((i & 15) == 15) {
#pragma unroll
        for (int u = 0; u < 8; ++u) { const float t1 = xs[u] + dppf<XOR8>(xs[u]), t2 = xs[u + 8] + dppf<XOR8>(xs[u + 8]); xs[u] = (c & 8) ? t2 : t1; }
#pragma unroll
        for (int u = 0; u < 4; ++u) { const float t1 = xs[u] + dppf<XOR7>(xs[u]), t2 = xs[u + 4] + dppf<XOR7>(xs[u + 4]); xs[u] = (c & 4) ? t2 : t1; }
#pragma unroll
        for (int u = 0; u < 2; ++u) { const float t1 = xs[u] + dppf<XOR2>(xs[u]), t2 = xs[u + 2] + dppf<XOR2>(xs[u + 2]); xs[u] = (c & 2) ? t2 : t1; }
        const float t1 = xs[0] + dppf<XOR1>(xs[0]), t2 = xs[1] + dppf<XOR1>(xs[1]);
        sc[0] = sc[1]; sc[1] = sc[2]; sc[2] = sc[3]; sc[3] = (c & 1) ? t2 : t1;
        PIN(sc[3]);
      }
      SB();
    });
  }
  {
    const float mw = wave_max(fmaxf(fmaxf(sc[0], sc[1]), fmaxf(sc[2], sc[3])));
    float l = 0.f;
#pragma unroll
    for (int r = 0; r < 4; ++r) l += __builtin_amdgcn_exp2f((sc[r] - mw) * C2);
    l = wave_sum(l);
    if (lane == 0) *reinterpret_cast<v2u*>(lds + L_ML + 8 * w) = v2u{__float_as_uint(mw), __float_as_uint(l)};
  }
  __syncthreads();
  float M, L;
  {
    const v2u ml = *reinterpret_cast<const v2u*>(lds + L_ML + 8 * (lane & 7));
    const float mu = __uint_as_float(ml.x);
    M = fmaxf(mu, dppf<XOR1>(mu)); M = fmaxf(M, dppf<XOR2>(M)); M = fmaxf(M, dppf<XOR7>(M));
    L = __uint_as_float(ml.y) * __builtin_amdgcn_exp2f((mu - M) * C2);
    L += dppf<XOR1>(L); L += dppf<XOR2>(L); L += dppf<XOR7>(L);
    if (tid == 0) __builtin_amdgcn_raw_buffer_store_b128(v4u{__float_as_uint(M), tag32, __float_as_uint(L), tag32}, rg, WS_ML + (b * 64 + j) * 16, 0, AUX_SC1);
  }
  unsigned pk[4];
#pragma unroll
  for (int r = 0; r < 4; ++r) pk[r] = split_hilo(__builtin_amdgcn_exp2f((sc[r] - M) * C2));
  SB();
  f32x4 oa = {0.f, 0.f, 0.f, 0.f};
  sfor<W>([&](auto I) {
    constexpr int i = I.v;
    oa = mfma(diagA<i & 15>(pk[i >> 4], sel), a[i], oa);
    PIN(oa); SB();
    a[i] = ldnt(rv, voff, i, xw ^ T8); SB();
    if constexpr (i < 16) { wr[i] = __builtin_amdgcn_raw_buffer_load_b128(ro, (lane >> 3) * (DM * 2) + (lane & 7) * 16, (i ^ ox) * 8 * (DM * 2), 0); SB(); }
  });
  v4u mlo = __builtin_amdgcn_raw_buffer_load_b128(rg, WS_ML + (b * 64 + (j ^ 1)) * 16, 0, AUX_SC1);
  SB();
  LAS unsigned char* const wst = (LAS unsigned char*)lds + L_W + w * 4096;
  int aw, ar, yo;
  sfor<W>([&](auto I) {
    constexpr int i = I.v;
    oa = mfma(diagA<i & 15>(pk[2 + (i >> 4)], sel), a[i], oa);
    PIN(oa); SB();
    if constexpr (i == 24) {
      aw = L_A + (2 * w + (g >> 1)) * A_STR + 16 * c + 8 * (g & 1); ar = L_A + c * A_STR + 16 * g; yo = WS_Y + (b * 64 + j) * 4096 + tid * 8;
      PIN(aw); PIN(ar); PIN(yo); SB();
    }
    if constexpr (i >= 8 && i < 24 && (i & 3) == 0) {
      constexpr int s = (i - 8) >> 2;
      sfor<4>([&](auto U) { *(LAS v4u*)(wst + (U.v ^ (ox & 3)) * 1024 + lane * 16) = wr[4 * s + U.v]; });
      sfor<4>([&](auto T) {
        LAS unsigned char* p = wst + (8 * g + (c >> 2)) * 128 + 32 * T.v + 8 * (c & 3);
        const v2u lo = __builtin_bit_cast(v2u, __builtin_amdgcn_ds_read_tr16_b64_v4i16((LAS s16x4*)p));
        const v2u hi = __builtin_bit_cast(v2u, __builtin_amdgcn_ds_read_tr16_b64_v4i16((LAS s16x4*)(p + 512)));
        wr[4 * s + T.v] = v4u{lo.x, lo.y, hi.x, hi.y};
      });
      SB();
    }
  });
  *reinterpret_cast<v2u*>(lds + aw) = v2u{cvtpk(oa.x, oa.y), cvtpk(oa.z, oa.w)};
  SB();
  if (!__all(mlo.y == tag32 && mlo.w == tag32)) {
    const unsigned char* src = ws + WS_ML + (b * 64 + (j ^ 1)) * 16;
#pragma unroll 1
    for (unsigned spins = 0;; ++spins) {
      asm volatile("global_load_dwordx4 %0, %1, off sc1\n\ts_waitcnt vmcnt(0)" : "=&v"(mlo) : "v"(src) : "memory");
      if (__all(mlo.y == tag32 && mlo.w == tag32)) break;
      if (spins > SPIN_CAP) { mlo.x = mlo.z = 0x7fc00000u; break; }
      __builtin_amdgcn_s_sleep(2);
    }
  }
  __syncthreads();
  {
    v4u A[4];
    sfor<4>([&](auto S) { A[S.v] = *reinterpret_cast<const v4u*>(lds + ar + 64 * (S.v ^ (ox >> 2))); });
    SB();
    PIN(mlo.x);
    const float Mo = __uint_as_float(mlo.x), Lo = __uint_as_float(mlo.z);
    const float Mh = fmaxf(M, Mo), e0 = __builtin_amdgcn_exp2f((M - Mh) * C2), e1 = __builtin_amdgcn_exp2f((Mo - Mh) * C2);
    const float cw = e0 / (e0 * L + e1 * Lo);
    f32x4 y[4] = {};
    sfor<4>([&](auto S) { sfor<4>([&](auto T) { y[T.v] = mfma(A[S.v], wr[4 * S.v + T.v], y[T.v]); }); });
    float v[4];
    sfor<4>([&](auto T) { v[T.v] = (y[T.v].x + y[T.v].y) + (y[T.v].z + y[T.v].w); });
    const float yc = swap16_add(swap32_add(v[0], v[2]), swap32_add(v[1], v[3])) * cw;
    const unsigned yb = gran(yc, t8), yn = dppu<XOR1>(yb);
    if (!(lane & 1)) __builtin_amdgcn_raw_buffer_store_b128(v4u{yb, tag32, yn, tag32}, rg, yo, 0, AUX_SC1);
  }
  if (w == 0) {
    const unsigned char* src = ws + WS_Y + (b * 64 + (lane >> 2)) * 4096 + j * 64 + (lane & 3) * 16;
    const unsigned char *s1 = src + 65536, *s2 = src + 2 * 65536, *s3 = src + 3 * 65536;
    v4u g0, g1, g2, g3;
#pragma unroll 1
    for (unsigned spins = 0;; ++spins) {
      asm volatile("global_load_dwordx4 %0, %4, off sc1\n\tglobal_load_dwordx4 %1, %5, off sc1\n\tglobal_load_dwordx4 %2, %6, off sc1\n\tglobal_load_dwordx4 %3, %7, off sc1\n\t"
                   "s_waitcnt vmcnt(0)"
                   : "=&v"(g0), "=&v"(g1), "=&v"(g2), "=&v"(g3) : "v"(src), "v"(s1), "v"(s2), "v"(s3) : "memory");
      if (__all((gbad(g0, tag32, t8) | gbad(g1, tag32, t8) | gbad(g2, tag32, t8) | gbad(g3, tag32, t8)) == 0)) break;
      if (spins > SPIN_CAP) { g0.x = g0.z = 0x7fc00000u; break; }
    }
    float r0 = (gval(g0.x) + gval(g1.x)) + (gval(g2.x) + gval(g3.x)), r1 = (gval(g0.z) + gval(g1.z)) + (gval(g2.z) + gval(g3.z));
    r0 += dppf<ROR4>(r0); r1 += dppf<ROR4>(r1);
    r0 += dppf<XOR8>(r0); r1 += dppf<XOR8>(r1);
    float r = swap16_add(r0, r1);
    r = swap32_add(r, r);
    if (lane < 32 && c < 4) out[b * DM + 8 * j + 2 * c + g] = (uint16_t)(cvtpk(r, 0.f) & 0xffffu);
    if (lane == 0 && j == 0) __hip_atomic_store((gu64*)(ws + WS_EPOCH + b * 256), tag, __ATOMIC_RELAXED, __HIP_MEMORY_SCOPE_AGENT);
  }
}

extern "C" void kernel_launch(void* const* d_in, const int* in_sizes, int n_in, void* d_out, int out_size, void* d_ws, size_t ws_size, hipStream_t stream) {
  (void)in_sizes; (void)n_in; (void)out_size; (void)ws_size;
  mha_decode_fused<<<NWG, NTHR, LDS_BYTES, stream>>>((const uint16_t*)d_in[0], (const uint16_t*)d_in[1], (const uint16_t*)d_in[2], (const uint16_t*)d_in[3],
                                                     (const uint16_t*)d_in[4], (unsigned char*)d_ws, (uint16_t*)d_out);
}
```

```cpp
#include <hip/hip_runtime.h>
#include <hip/hip_bf16.h>
#include <cstdint>

typedef unsigned v4u __attribute__((ext_vector_type(4)));
typedef unsigned v2u __attribute__((ext_vector_type(2)));
typedef float f32x4 __attribute__((ext_vector_type(4)));
typedef short bf16x8 __attribute__((ext_vector_type(8)));
typedef short s16x4 __attribute__((ext_vector_type(4)));
typedef __bf16 bf16x2 __attribute__((ext_vector_type(2)));
typedef __attribute__((address_space(1))) unsigned long long gu64;
#define LAS __attribute__((address_space(3)))

constexpr int DM = 512, NH = 32, SK = 4096, DH = 128;
constexpr int NWG = 256, NWAVE = 8, NTHR = NWAVE * 64;
constexpr int KEYS_WG = SK / 2;
constexpr int W = 32;
constexpr int ROW = NWAVE * 4096;
constexpr float C2 = 0.088388347648318440f * 1.4426950408889634f;
constexpr int RSRC_W3 = 0x00020000, AUX_NT = 2, AUX_SC1 = 16;
constexpr unsigned SPIN_CAP = 1u << 18;
constexpr int WS_EPOCH = 0, WS_ML = 4096, WS_Y = 8192, WS_END = WS_Y + NWG * 4096;
constexpr int A_STR = 272;
constexpr int L_Q = 0;
constexpr int L_ML = L_Q + NWAVE * 512;
constexpr int L_A = L_ML + 64;
constexpr int L_W = L_A + 16 * A_STR;
constexpr int LDS_BYTES = L_W + NWAVE * 4096;

__device__ __forceinline__ f32x4 mfma(v4u a, v4u b, f32x4 c) {
  return __builtin_amdgcn_mfma_f32_16x16x32_bf16(__builtin_bit_cast(bf16x8, a), __builtin_bit_cast(bf16x8, b), c, 0, 0, 0);
}
__device__ __forceinline__ float dot2(unsigned k, unsigned q, float acc) {
  return __builtin_amdgcn_fdot2_f32_bf16(__builtin_bit_cast(bf16x2, k), __builtin_bit_cast(bf16x2, q), acc, false);
}
template <int CTRL> __device__ __forceinline__ unsigned dppu(unsigned x) { return (unsigned)__builtin_amdgcn_mov_dpp((int)x, CTRL, 0xf, 0xf, true); }
template <int CTRL> __device__ __forceinline__ float dppf(float x) { return __builtin_bit_cast(float, dppu<CTRL>(__builtin_bit_cast(unsigned, x))); }
constexpr int XOR1 = 0xB1, XOR2 = 0x4E, XOR7 = 0x141, XOR8 = 0x128, ROR4 = 0x124, BCAST = 0x150;
__device__ __forceinline__ unsigned cvtpk(float lo, float hi) {
  bf16x2 r = {(__bf16)lo, (__bf16)hi};
  return __builtin_bit_cast(unsigned, r);
}
__device__ __forceinline__ unsigned split_hilo(float x) {
  const unsigned h = cvtpk(x, 0.f);
  return cvtpk(__builtin_bit_cast(float, h << 16), x - __builtin_bit_cast(float, h << 16));
}
__device__ __forceinline__ float swap16_add(float a, float b) {
  const auto s = __builtin_amdgcn_permlane16_swap(__float_as_uint(a), __float_as_uint(b), false, false);
  return __uint_as_float(s[0]) + __uint_as_float(s[1]);
}
__device__ __forceinline__ float swap32_add(float a, float b) {
  const auto s = __builtin_amdgcn_permlane32_swap(__float_as_uint(a), __float_as_uint(b), false, false);
  return __uint_as_float(s[0]) + __uint_as_float(s[1]);
}
__device__ __forceinline__ float wave_max(float x) {
  x = fmaxf(x, dppf<XOR1>(x)); x = fmaxf(x, dppf<XOR2>(x)); x = fmaxf(x, dppf<XOR7>(x)); x = fmaxf(x, dppf<XOR8>(x));
  auto s = __builtin_amdgcn_permlane16_swap(__float_as_uint(x), __float_as_uint(x), false, false);
  x = fmaxf(__uint_as_float(s[0]), __uint_as_float(s[1]));
  auto t = __builtin_amdgcn_permlane32_swap(__float_as_uint(x), __float_as_uint(x), false, false);
  return fmaxf(__uint_as_float(t[0]), __uint_as_float(t[1]));
}
__device__ __forceinline__ float wave_sum(float x) {
  x += dppf<XOR1>(x); x += dppf<XOR2>(x); x += dppf<XOR7>(x); x += dppf<XOR8>(x);
  x = swap16_add(x, x);
  return swap32_add(x, x);
}
template <int SRC> __device__ __forceinline__ v4u diagA(unsigned pk, const v4u& sel) {
  const unsigned t = dppu<BCAST + SRC>(pk);
  v4u a;
  a.x = __builtin_amdgcn_perm(t, 0u, sel.x); a.y = __builtin_amdgcn_perm(t, 0u, sel.y);
  a.z = __builtin_amdgcn_perm(t, 0u, sel.z); a.w = __builtin_amdgcn_perm(t, 0u, sel.w);
  return a;
}
__device__ __forceinline__ unsigned gran(float v, unsigned t8) { return (__float_as_uint(v) & 0xffffff00u) | t8; }
__device__ __forceinline__ float gval(unsigned x) { return __uint_as_float(x & 0xffffff00u); }
__device__ __forceinline__ unsigned gbad(const v4u& g, unsigned tag32, unsigned t8) { return (g.y ^ tag32) | (g.w ^ tag32) | (((g.x ^ t8) | (g.z ^ t8)) & 0xffu); }

#define SB() __builtin_amdgcn_sched_barrier(0)
#define PIN(x) asm volatile("" : "+v"(x) :: "memory")
template <int I> struct IC { static constexpr int v = I; };
template <int N, int I = 0, typename F> __device__ __forceinline__ void sfor(F&& f) {
  if constexpr (I < N) { f(IC<I>{}); sfor<N, I + 1>(f); }
}
__device__ __forceinline__ v4u ldnt(__amdgpu_buffer_rsrc_t r, int voff, int i, int xo) {
  return __builtin_amdgcn_raw_buffer_load_b128(r, voff + (i & 3) * 1024, xo ^ ((i >> 2) * ROW), AUX_NT);
}

__global__ void __launch_bounds__(NTHR, 2) mha_decode_fused(const uint16_t* __restrict__ x, const uint16_t* __restrict__ kc, const uint16_t* __restrict__ vc,
                                                            const uint16_t* __restrict__ wq, const uint16_t* __restrict__ wo, unsigned char* ws, uint16_t* out) {
  extern __shared__ __attribute__((aligned(16))) unsigned char lds[];
  const unsigned long long T0 = __builtin_amdgcn_s_memrealtime();
  const int tid = threadIdx.x, lane = tid & 63, w = __builtin_amdgcn_readfirstlane(tid >> 6), g = lane >> 4, c = lane & 15;
  const int bid = blockIdx.x, h = (bid & 7) * 4 + (bid >> 6), sub = (bid >> 3) & 7, b = sub >> 1, half = sub & 1;
  const int j = h * 2 + half;
  const int wv = w ^ sub, ix = (sub & 3) << 2, ox = sub << 1;
  const int xw = (w << 12) ^ ((h & 1) * (1 << 14 | 1 << 18)) ^ (half << 17), T8 = 8 * ROW;
  const size_t kvoff = ((size_t)(b * NH + h) * SK + half * KEYS_WG) * DH;
  const __amdgpu_buffer_rsrc_t rk = __builtin_amdgcn_make_buffer_rsrc((void*)(kc + kvoff), 0, KEYS_WG * DH * 2, RSRC_W3);
  const __amdgpu_buffer_rsrc_t rv = __builtin_amdgcn_make_buffer_rsrc((void*)(vc + kvoff), 0, KEYS_WG * DH * 2, RSRC_W3);
  const __amdgpu_buffer_rsrc_t rq = __builtin_amdgcn_make_buffer_rsrc((void*)(wq + (size_t)(64 * wv) * (NH * DH) + h * DH), 0, 63 * NH * DH * 2 + 256, RSRC_W3);
  const __amdgpu_buffer_rsrc_t ro = __builtin_amdgcn_make_buffer_rsrc((void*)(wo + (size_t)(h * DH) * DM + 64 * w), 0, 127 * DM * 2 + 128, RSRC_W3);
  const __amdgpu_buffer_rsrc_t rg = __builtin_amdgcn_make_buffer_rsrc((void*)ws, 0, WS_END, RSRC_W3);
  const int voff = lane * 16;
  const unsigned long long epoch = __hip_atomic_load((gu64*)(ws + WS_EPOCH + b * 256), __ATOMIC_RELAXED, __HIP_MEMORY_SCOPE_AGENT);
  const unsigned xv = x[b * DM + 64 * wv + 4 * (c ^ ix) + g];
  v4u a[W], wr[16];
  sfor<16>([&](auto I) {
    a[16 + I.v] = __builtin_amdgcn_raw_buffer_load_b128(rq, g * (NH * DH * 2) + c * 16, (I.v ^ ix) * 4 * (NH * DH * 2), 0); SB();
    a[I.v] = ldnt(rk, voff, I.v, xw); SB();
  });
  v4u sel;
  {
    const unsigned src = (c < 8) ? 0x0504u : 0x0706u;
    const unsigned full = (c & 1) ? (src << 16 | 0x0c0cu) : (0x0c0c0000u | src);
    const int d = (c & 7) >> 1;
    sel.x = d == 0 ? full : 0x0c0c0c0cu; sel.y = d == 1 ? full : 0x0c0c0c0cu; sel.z = d == 2 ? full : 0x0c0c0c0cu; sel.w = d == 3 ? full : 0x0c0c0c0cu;
  }
  {
    f32x4 qa = {0.f, 0.f, 0.f, 0.f};
    sfor<16>([&](auto I) {
      qa = mfma(diagA<I.v>(xv, sel), a[16 + I.v], qa);
      PIN(qa); SB();
      a[16 + I.v] = ldnt(rk, voff, 16 + I.v, xw); SB();
    });
    if (g < 2) *reinterpret_cast<f32x4*>(lds + L_Q + w * 512 + 32 * c + 16 * g) = qa;
  }
  const unsigned long long tag = ((unsigned long long)(unsigned)__builtin_amdgcn_readfirstlane((unsigned)(epoch >> 32)) << 32 |
                                  (unsigned)__builtin_amdgcn_readfirstlane((unsigned)epoch)) + 1;
  const unsigned tag32 = (unsigned)tag, t8 = (unsigned)(tag >> 32) & 0xffu;
  __syncthreads();
  v4u qp;
  {
    f32x4 s0 = *reinterpret_cast<const f32x4*>(lds + L_Q + 32 * c), s1 = *reinterpret_cast<const f32x4*>(lds + L_Q + 32 * c + 16);
#pragma unroll
    for (int u = 1; u < NWAVE; ++u) {
      s0 += *reinterpret_cast<const f32x4*>(lds + L_Q + u * 512 + 32 * c);
      s1 += *reinterpret_cast<const f32x4*>(lds + L_Q + u * 512 + 32 * c + 16);
    }
    qp.x = cvtpk(s0.x, s0.y); qp.y = cvtpk(s0.z, s0.w); qp.z = cvtpk(s1.x, s1.y); qp.w = cvtpk(s1.z, s1.w);
  }
  SB();
  float sc[4] = {0.f, 0.f, 0.f, 0.f};
#pragma unroll 1
  for (int it = 0; it < 2; ++it) {
    const __amdgpu_buffer_rsrc_t rn = it ? rv : rk;
    const int xo = it ? xw : xw ^ T8;
    float xs[16];
    sfor<W>([&](auto I) {
      constexpr int i = I.v;
      float d = dot2(a[i].x, qp.x, 0.f); d = dot2(a[i].y, qp.y, d); d = dot2(a[i].z, qp.z, d); d = dot2(a[i].w, qp.w, d);
      PIN(d);
      xs[i & 15] = d;
      SB();
      a[i] = ldnt(rn, voff, i, xo);
      if constexpr ((i & 15) == 15) {
#pragma unroll
        for (int u = 0; u < 8; ++u) { const float t1 = xs[u] + dppf<XOR8>(xs[u]), t2 = xs[u + 8] + dppf<XOR8>(xs[u + 8]); xs[u] = (c & 8) ? t2 : t1; }
#pragma unroll
        for (int u = 0; u < 4; ++u) { const float t1 = xs[u] + dppf<XOR7>(xs[u]), t2 = xs[u + 4] + dppf<XOR7>(xs[u + 4]); xs[u] = (c & 4) ? t2 : t1; }
#pragma unroll
        for (int u = 0; u < 2; ++u) { const float t1 = xs[u] + dppf<XOR2>(xs[u]), t2 = xs[u + 2] + dppf<XOR2>(xs[u + 2]); xs[u] = (c & 2) ? t2 : t1; }
        const float t1 = xs[0] + dppf<XOR1>(xs[0]), t2 = xs[1] + dppf<XOR1>(xs[1]);
        sc[0] = sc[1]; sc[1] = sc[2]; sc[2] = sc[3]; sc[3] = (c & 1) ? t2 : t1;
        PIN(sc[3]);
      }
      SB();
    });
  }
  {
    const float mw = wave_max(fmaxf(fmaxf(sc[0], sc[1]), fmaxf(sc[2], sc[3])));
    float l = 0.f;
#pragma unroll
    for (int r = 0; r < 4; ++r) l += __builtin_amdgcn_exp2f((sc[r] - mw) * C2);
    l = wave_sum(l);
    if (lane == 0) *reinterpret_cast<v2u*>(lds + L_ML + 8 * w) = v2u{__float_as_uint(mw), __float_as_uint(l)};
  }
  __syncthreads();
  float M, L;
  {
    const v2u ml = *reinterpret_cast<const v2u*>(lds + L_ML + 8 * (lane & 7));
    const float mu = __uint_as_float(ml.x);
    M = fmaxf(mu, dppf<XOR1>(mu)); M = fmaxf(M, dppf<XOR2>(M)); M = fmaxf(M, dppf<XOR7>(M));
    L = __uint_as_float(ml.y) * __builtin_amdgcn_exp2f((mu - M) * C2);
    L += dppf<XOR1>(L); L += dppf<XOR2>(L); L += dppf<XOR7>(L);
    if (tid == 0) __builtin_amdgcn_raw_buffer_store_b128(v4u{__float_as_uint(M), tag32, __float_as_uint(L), tag32}, rg, WS_ML + (b * 64 + j) * 16, 0, AUX_SC1);
  }
  unsigned pk[4];
#pragma unroll
  for (int r = 0; r < 4; ++r) pk[r] = split_hilo(__builtin_amdgcn_exp2f((sc[r] - M) * C2));
  SB();
  f32x4 oa = {0.f, 0.f, 0.f, 0.f};
  sfor<W>([&](auto I) {
    constexpr int i = I.v;
    oa = mfma(diagA<i & 15>(pk[i >> 4], sel), a[i], oa);
    PIN(oa); SB();
    a[i] = ldnt(rv, voff, i, xw ^ T8); SB();
    if constexpr (i < 16) { wr[i] = __builtin_amdgcn_raw_buffer_load_b128(ro, (lane >> 3) * (DM * 2) + (lane & 7) * 16, (i ^ ox) * 8 * (DM * 2), 0); SB(); }
  });
  v4u mlo = __builtin_amdgcn_raw_buffer_load_b128(rg, WS_ML + (b * 64 + (j ^ 1)) * 16, 0, AUX_SC1);
  SB();
  LAS unsigned char* const wst = (LAS unsigned char*)lds + L_W + w * 4096;
  int aw, ar, yo;
  sfor<W>([&](auto I) {
    constexpr int i = I.v;
    oa = mfma(diagA<i & 15>(pk[2 + (i >> 4)], sel), a[i], oa);
    PIN(oa); SB();
    if constexpr (i == 24) {
      aw = L_A + (2 * w + (g >> 1)) * A_STR + 16 * c + 8 * (g & 1); ar = L_A + c * A_STR + 16 * g; yo = WS_Y + (b * 64 + j) * 4096 + tid * 8;
      PIN(aw); PIN(ar); PIN(yo); SB();
    }
    if constexpr (i >= 8 && i < 24 && (i & 3) == 0) {
      constexpr int s = (i - 8) >> 2;
      sfor<4>([&](auto U) { *(LAS v4u*)(wst + (U.v ^ (ox & 3)) * 1024 + lane * 16) = wr[4 * s + U.v]; });
      sfor<4>([&](auto T) {
        LAS unsigned char* p = wst + (8 * g + (c >> 2)) * 128 + 32 * T.v + 8 * (c & 3);
        const v2u lo = __builtin_bit_cast(v2u, __builtin_amdgcn_ds_read_tr16_b64_v4i16((LAS s16x4*)p));
        const v2u hi = __builtin_bit_cast(v2u, __builtin_amdgcn_ds_read_tr16_b64_v4i16((LAS s16x4*)(p + 512)));
        wr[4 * s + T.v] = v4u{lo.x, lo.y, hi.x, hi.y};
      });
      SB();
    }
  });
  *reinterpret_cast<v2u*>(lds + aw) = v2u{cvtpk(oa.x, oa.y), cvtpk(oa.z, oa.w)};
  SB();
  if (!__all(mlo.y == tag32 && mlo.w == tag32)) {
    const unsigned char* src = ws + WS_ML + (b * 64 + (j ^ 1)) * 16;
#pragma unroll 1
    for (unsigned spins = 0;; ++spins) {
      asm volatile("global_load_dwordx4 %0, %1, off sc1\n\ts_waitcnt vmcnt(0)" : "=&v"(mlo) : "v"(src) : "memory");
      if (__all(mlo.y == tag32 && mlo.w == tag32)) break;
      if (spins > SPIN_CAP) { mlo.x = mlo.z = 0x7fc00000u; break; }
      __builtin_amdgcn_s_sleep(2);
    }
  }
  __syncthreads();
  {
    v4u A[4];
    sfor<4>([&](auto S) { A[S.v] = *reinterpret_cast<const v4u*>(lds + ar + 64 * (S.v ^ (ox >> 2))); });
    SB();
    PIN(mlo.x);
    const float Mo = __uint_as_float(mlo.x), Lo = __uint_as_float(mlo.z);
    const float Mh = fmaxf(M, Mo), e0 = __builtin_amdgcn_exp2f((M - Mh) * C2), e1 = __builtin_amdgcn_exp2f((Mo - Mh) * C2);
    const float cw = e0 / (e0 * L + e1 * Lo);
    f32x4 y[4] = {};
    sfor<4>([&](auto S) { sfor<4>([&](auto T) { y[T.v] = mfma(A[S.v], wr[4 * S.v + T.v], y[T.v]); }); });
    float v[4];
    sfor<4>([&](auto T) { v[T.v] = (y[T.v].x + y[T.v].y) + (y[T.v].z + y[T.v].w); });
    const float yc = swap16_add(swap32_add(v[0], v[2]), swap32_add(v[1], v[3])) * cw;
    const unsigned yb = gran(yc, t8), yn = dppu<XOR1>(yb);
    if (!(lane & 1)) __builtin_amdgcn_raw_buffer_store_b128(v4u{yb, tag32, yn, tag32}, rg, yo, 0, AUX_SC1);
  }
  if (w == 0) {
    const unsigned char* src = ws + WS_Y + (b * 64 + (lane >> 2)) * 4096 + j * 64 + (lane & 3) * 16;
    const unsigned char *s1 = src + 65536, *s2 = src + 2 * 65536, *s3 = src + 3 * 65536;
    v4u g0, g1, g2, g3;
#pragma unroll 1
    for (unsigned spins = 0;; ++spins) {
      asm volatile("global_load_dwordx4 %0, %4, off sc1\n\tglobal_load_dwordx4 %1, %5, off sc1\n\tglobal_load_dwordx4 %2, %6, off sc1\n\tglobal_load_dwordx4 %3, %7, off sc1\n\t"
                   "s_waitcnt vmcnt(0)"
                   : "=&v"(g0), "=&v"(g1), "=&v"(g2), "=&v"(g3) : "v"(src), "v"(s1), "v"(s2), "v"(s3) : "memory");
      if (__all((gbad(g0, tag32, t8) | gbad(g1, tag32, t8) | gbad(g2, tag32, t8) | gbad(g3, tag32, t8)) == 0)) break;
      if (spins > SPIN_CAP) { g0.x = g0.z = 0x7fc00000u; break; }
    }
    float r0 = (gval(g0.x) + gval(g1.x)) + (gval(g2.x) + gval(g3.x)), r1 = (gval(g0.z) + gval(g1.z)) + (gval(g2.z) + gval(g3.z));
    r0 += dppf<ROR4>(r0); r1 += dppf<ROR4>(r1);
    r0 += dppf<XOR8>(r0); r1 += dppf<XOR8>(r1);
    float r = swap16_add(r0, r1);
    r = swap32_add(r, r);
    if (lane < 32 && c < 4) out[b * DM + 8 * j + 2 * c + g] = (uint16_t)(cvtpk(r, 0.f) & 0xffffu);
    if (lane == 0 && j == 0) __hip_atomic_store((gu64*)(ws + WS_EPOCH + b * 256), tag, __ATOMIC_RELAXED, __HIP_MEMORY_SCOPE_AGENT);
    if (bid == 0) {
      const unsigned long long T1 = __builtin_amdgcn_s_memrealtime();
      const unsigned long long code = (unsigned long long)(__builtin_amdgcn_s_getreg(20 | (3  __H0__ )) & 0xf);
      const unsigned long long until = T1 + 10000ull - 3600ull + 1000ull * code;
      while (__builtin_amdgcn_s_memrealtime() < until) __builtin_amdgcn_s_sleep(1);
    }
  }
}

extern "C" void kernel_launch(void* const* d_in, const int* in_sizes, int n_in, void* d_out, int out_size, void* d_ws, size_t ws_size, hipStream_t stream) {
  (void)in_sizes; (void)n_in; (void)out_size; (void)ws_size;
  mha_decode_fused<<<NWG, NTHR, LDS_BYTES, stream>>>((const uint16_t*)d_in[0], (const uint16_t*)d_in[1], (const uint16_t*)d_in[2], (const uint16_t*)d_in[3],
                                                     (const uint16_t*)d_in[4], (unsigned char*)d_ws, (uint16_t*)d_out);
}
```

```cpp
#include <hip/hip_runtime.h>
#include <hip/hip_bf16.h>
#include <cstdint>

typedef unsigned v4u __attribute__((ext_vector_type(4)));
typedef unsigned v2u __attribute__((ext_vector_type(2)));
typedef float f32x4 __attribute__((ext_vector_type(4)));
typedef short bf16x8 __attribute__((ext_vector_type(8)));
typedef short s16x4 __attribute__((ext_vector_type(4)));
typedef __bf16 bf16x2 __attribute__((ext_vector_type(2)));
typedef __attribute__((address_space(1))) unsigned long long gu64;
#define LAS __attribute__((address_space(3)))

constexpr int DM = 512, NH = 32, SK = 4096, DH = 128;
constexpr int NWG = 256, NWAVE = 8, NTHR = NWAVE * 64;
constexpr int KEYS_WG = SK / 2;
constexpr int W = 32;
constexpr int ROW = NWAVE * 4096;
constexpr float C2 = 0.088388347648318440f * 1.4426950408889634f;
constexpr int RSRC_W3 = 0x00020000, AUX_NT = 2, AUX_SC1 = 16;
constexpr unsigned SPIN_CAP = 1u << 18;
constexpr int WS_EPOCH = 0, WS_ML = 4096, WS_Y = 8192, WS_END = WS_Y + NWG * 4096;
constexpr int A_STR = 272;
constexpr int L_Q = 0;
constexpr int L_ML = L_Q + NWAVE * 512;
constexpr int L_A = L_ML + 64;
constexpr int L_W = L_A + 16 * A_STR;
constexpr int LDS_BYTES = L_W + NWAVE * 4096;

__device__ __forceinline__ f32x4 mfma(v4u a, v4u b, f32x4 c) {
  return __builtin_amdgcn_mfma_f32_16x16x32_bf16(__builtin_bit_cast(bf16x8, a), __builtin_bit_cast(bf16x8, b), c, 0, 0, 0);
}
__device__ __forceinline__ float dot2(unsigned k, unsigned q, float acc) {
  return __builtin_amdgcn_fdot2_f32_bf16(__builtin_bit_cast(bf16x2, k), __builtin_bit_cast(bf16x2, q), acc, false);
}
template <int CTRL> __device__ __forceinline__ unsigned dppu(unsigned x) { return (unsigned)__builtin_amdgcn_mov_dpp((int)x, CTRL, 0xf, 0xf, true); }
template <int CTRL> __device__ __forceinline__ float dppf(float x) { return __builtin_bit_cast(float, dppu<CTRL>(__builtin_bit_cast(unsigned, x))); }
constexpr int XOR1 = 0xB1, XOR2 = 0x4E, XOR7 = 0x141, XOR8 = 0x128, ROR4 = 0x124, BCAST = 0x150;
__device__ __forceinline__ unsigned cvtpk(float lo, float hi) {
  bf16x2 r = {(__bf16)lo, (__bf16)hi};
  return __builtin_bit_cast(unsigned, r);
}
__device__ __forceinline__ unsigned split_hilo(float x) {
  const unsigned h = cvtpk(x, 0.f);
  return cvtpk(__builtin_bit_cast(float, h << 16), x - __builtin_bit_cast(float, h << 16));
}
__device__ __forceinline__ float swap16_add(float a, float b) {
  const auto s = __builtin_amdgcn_permlane16_swap(__float_as_uint(a), __float_as_uint(b), false, false);
  return __uint_as_float(s[0]) + __uint_as_float(s[1]);
}
__device__ __forceinline__ float swap32_add(float a, float b) {
  const auto s = __builtin_amdgcn_permlane32_swap(__float_as_uint(a), __float_as_uint(b), false, false);
  return __uint_as_float(s[0]) + __uint_as_float(s[1]);
}
__device__ __forceinline__ float wave_max(float x) {
  x = fmaxf(x, dppf<XOR1>(x)); x = fmaxf(x, dppf<XOR2>(x)); x = fmaxf(x, dppf<XOR7>(x)); x = fmaxf(x, dppf<XOR8>(x));
  auto s = __builtin_amdgcn_permlane16_swap(__float_as_uint(x), __float_as_uint(x), false, false);
  x = fmaxf(__uint_as_float(s[0]), __uint_as_float(s[1]));
  auto t = __builtin_amdgcn_permlane32_swap(__float_as_uint(x), __float_as_uint(x), false, false);
  return fmaxf(__uint_as_float(t[0]), __uint_as_float(t[1]));
}
__device__ __forceinline__ float wave_sum(float x) {
  x += dppf<XOR1>(x); x += dppf<XOR2>(x); x += dppf<XOR7>(x); x += dppf<XOR8>(x);
  x = swap16_add(x, x);
  return swap32_add(x, x);
}
template <int SRC> __device__ __forceinline__ v4u diagA(unsigned pk, const v4u& sel) {
  const unsigned t = dppu<BCAST + SRC>(pk);
  v4u a;
  a.x = __builtin_amdgcn_perm(t, 0u, sel.x); a.y = __builtin_amdgcn_perm(t, 0u, sel.y);
  a.z = __builtin_amdgcn_perm(t, 0u, sel.z); a.w = __builtin_amdgcn_perm(t, 0u, sel.w);
  return a;
}
__device__ __forceinline__ unsigned gran(float v, unsigned t8) { return (__float_as_uint(v) & 0xffffff00u) | t8; }
__device__ __forceinline__ float gval(unsigned x) { return __uint_as_float(x & 0xffffff00u); }
__device__ __forceinline__ unsigned gbad(const v4u& g, unsigned tag32, unsigned t8) { return (g.y ^ tag32) | (g.w ^ tag32) | (((g.x ^ t8) | (g.z ^ t8)) & 0xffu); }

#define SB() __builtin_amdgcn_sched_barrier(0)
#define PIN(x) asm volatile("" : "+v"(x) :: "memory")
template <int I> struct IC { static constexpr int v = I; };
template <int N, int I = 0, typename F> __device__ __forceinline__ void sfor(F&& f) {
  if constexpr (I < N) { f(IC<I>{}); sfor<N, I + 1>(f); }
}
__device__ __forceinline__ v4u ldnt(__amdgpu_buffer_rsrc_t r, int voff, int i, int xo) {
  return __builtin_amdgcn_raw_buffer_load_b128(r, voff + (i & 3) * 1024, xo ^ ((i >> 2) * ROW), AUX_NT);
}

__global__ void __launch_bounds__(NTHR, 2) mha_decode_fused(const uint16_t* __restrict__ x, const uint16_t* __restrict__ kc, const uint16_t* __restrict__ vc,
                                                            const uint16_t* __restrict__ wq, const uint16_t* __restrict__ wo, unsigned char* ws, uint16_t* out) {
  extern __shared__ __attribute__((aligned(16))) unsigned char lds[];
  const int tid = threadIdx.x, lane = tid & 63, w = __builtin_amdgcn_readfirstlane(tid >> 6), g = lane >> 4, c = lane & 15;
  const int bid = blockIdx.x, h = (bid & 7) * 4 + (bid >> 6), sub = (bid >> 3) & 7, b = sub >> 1, half = sub & 1;
  const int j = h * 2 + half;
  const int wv = w ^ sub, ix = (sub & 3) << 2, ox = sub << 1;
  const int xw = (w << 12) ^ ((h & 1) * (1 << 14 | 1 << 18)) ^ (half << 17), T8 = 8 * ROW;
  const size_t kvoff = ((size_t)(b * NH + h) * SK + half * KEYS_WG) * DH;
  const __amdgpu_buffer_rsrc_t rk = __builtin_amdgcn_make_buffer_rsrc((void*)(kc + kvoff), 0, KEYS_WG * DH * 2, RSRC_W3);
  const __amdgpu_buffer_rsrc_t rv = __builtin_amdgcn_make_buffer_rsrc((void*)(vc + kvoff), 0, KEYS_WG * DH * 2, RSRC_W3);
  const __amdgpu_buffer_rsrc_t rq = __builtin_amdgcn_make_buffer_rsrc((void*)(wq + (size_t)(64 * wv) * (NH * DH) + h * DH), 0, 63 * NH * DH * 2 + 256, RSRC_W3);
  const __amdgpu_buffer_rsrc_t ro = __builtin_amdgcn_make_buffer_rsrc((void*)(wo + (size_t)(h * DH) * DM + 64 * w), 0, 127 * DM * 2 + 128, RSRC_W3);
  const __amdgpu_buffer_rsrc_t rg = __builtin_amdgcn_make_buffer_rsrc((void*)ws, 0, WS_END, RSRC_W3);
  const int voff = lane * 16;
  v4u a[W], wr[16];
  unsigned xv;
  sfor<16>([&](auto I) {
    a[I.v] = ldnt(rk, voff, I.v, xw); SB();
    a[16 + I.v] = __builtin_amdgcn_raw_buffer_load_b128(rq, g * (NH * DH * 2) + c * 16, (I.v ^ ix) * 4 * (NH * DH * 2), 0); SB();
    if constexpr (I.v == 3) { xv = x[b * DM + 64 * wv + 4 * (c ^ ix) + g]; SB(); }
  });
  const unsigned long long epoch = __hip_atomic_load((gu64*)(ws + WS_EPOCH + b * 256), __ATOMIC_RELAXED, __HIP_MEMORY_SCOPE_AGENT);
  SB();
  v4u sel;
  {
    const unsigned src = (c < 8) ? 0x0504u : 0x0706u;
    const unsigned full = (c & 1) ? (src << 16 | 0x0c0cu) : (0x0c0c0000u | src);
    const int d = (c & 7) >> 1;
    sel.x = d == 0 ? full : 0x0c0c0c0cu; sel.y = d == 1 ? full : 0x0c0c0c0cu; sel.z = d == 2 ? full : 0x0c0c0c0cu; sel.w = d == 3 ? full : 0x0c0c0c0cu;
  }
  {
    f32x4 qa = {0.f, 0.f, 0.f, 0.f};
    sfor<16>([&](auto I) {
      qa = mfma(diagA<I.v>(xv, sel), a[16 + I.v], qa);
      PIN(qa); SB();
      a[16 + I.v] = ldnt(rk, voff, 16 + I.v, xw); SB();
    });
    if (g < 2) *reinterpret_cast<f32x4*>(lds + L_Q + w * 512 + 32 * c + 16 * g) = qa;
  }
  const unsigned long long tag = ((unsigned long long)(unsigned)__builtin_amdgcn_readfirstlane((unsigned)(epoch >> 32)) << 32 |
                                  (unsigned)__builtin_amdgcn_readfirstlane((unsigned)epoch)) + 1;
  const unsigned tag32 = (unsigned)tag, t8 = (unsigned)(tag >> 32) & 0xffu;
  __syncthreads();
  v4u qp;
  {
    f32x4 s0 = *reinterpret_cast<const f32x4*>(lds + L_Q + 32 * c), s1 = *reinterpret_cast<const f32x4*>(lds + L_Q + 32 * c + 16);
#pragma unroll
    for (int u = 1; u < NWAVE; ++u) {
      s0 += *reinterpret_cast<const f32x4*>(lds + L_Q + u * 512 + 32 * c);
      s1 += *reinterpret_cast<const f32x4*>(lds + L_Q + u * 512 + 32 * c + 16);
    }
    qp.x = cvtpk(s0.x, s0.y); qp.y = cvtpk(s0.z, s0.w); qp.z = cvtpk(s1.x, s1.y); qp.w = cvtpk(s1.z, s1.w);
  }
  SB();
  float sc[4] = {0.f, 0.f, 0.f, 0.f};
#pragma unroll 1
  for (int it = 0; it < 2; ++it) {
    const __amdgpu_buffer_rsrc_t rn = it ? rv : rk;
    const int xo = it ? xw : xw ^ T8;
    float xs[16];
    sfor<W>([&](auto I) {
      constexpr int i = I.v;
      float d = dot2(a[i].x, qp.x, 0.f); d = dot2(a[i].y, qp.y, d); d = dot2(a[i].z, qp.z, d); d = dot2(a[i].w, qp.w, d);
      PIN(d);
      xs[i & 15] = d;
      SB();
      a[i] = ldnt(rn, voff, i, xo);
      if constexpr ((i & 15) == 15) {
#pragma unroll
        for (int u = 0; u < 8; ++u) { const float t1 = xs[u] + dppf<XOR8>(xs[u]), t2 = xs[u + 8] + dppf<XOR8>(xs[u + 8]); xs[u] = (c & 8) ? t2 : t1; }
#pragma unroll
        for (int u = 0; u < 4; ++u) { const float t1 = xs[u] + dppf<XOR7>(xs[u]), t2 = xs[u + 4] + dppf<XOR7>(xs[u + 4]); xs[u] = (c & 4) ? t2 : t1; }
#pragma unroll
        for (int u = 0; u < 2; ++u) { const float t1 = xs[u] + dppf<XOR2>(xs[u]), t2 = xs[u + 2] + dppf<XOR2>(xs[u + 2]); xs[u] = (c & 2) ? t2 : t1; }
        const float t1 = xs[0] + dppf<XOR1>(xs[0]), t2 = xs[1] + dppf<XOR1>(xs[1]);
        sc[0] = sc[1]; sc[1] = sc[2]; sc[2] = sc[3]; sc[3] = (c & 1) ? t2 : t1;
        PIN(sc[3]);
      }
      SB();
    });
  }
  {
    const float mw = wave_max(fmaxf(fmaxf(sc[0], sc[1]), fmaxf(sc[2], sc[3])));
    float l = 0.f;
#pragma unroll
    for (int r = 0; r < 4; ++r) l += __builtin_amdgcn_exp2f((sc[r] - mw) * C2);
    l = wave_sum(l);
    if (lane == 0) *reinterpret_cast<v2u*>(lds + L_ML + 8 * w) = v2u{__float_as_uint(mw), __float_as_uint(l)};
  }
  __syncthreads();
  float M, L;
  {
    const v2u ml = *reinterpret_cast<const v2u*>(lds + L_ML + 8 * (lane & 7));
    const float mu = __uint_as_float(ml.x);
    M = fmaxf(mu, dppf<XOR1>(mu)); M = fmaxf(M, dppf<XOR2>(M)); M = fmaxf(M, dppf<XOR7>(M));
    L = __uint_as_float(ml.y) * __builtin_amdgcn_exp2f((mu - M) * C2);
    L += dppf<XOR1>(L); L += dppf<XOR2>(L); L += dppf<XOR7>(L);
    if (tid == 0) __builtin_amdgcn_raw_buffer_store_b128(v4u{__float_as_uint(M), tag32, __float_as_uint(L), tag32}, rg, WS_ML + (b * 64 + j) * 16, 0, AUX_SC1);
  }
  unsigned pk[4];
#pragma unroll
  for (int r = 0; r < 4; ++r) pk[r] = split_hilo(__builtin_amdgcn_exp2f((sc[r] - M) * C2));
  SB();
  f32x4 oa = {0.f, 0.f, 0.f, 0.f};
  sfor<W>([&](auto I) {
    constexpr int i = I.v;
    oa = mfma(diagA<i & 15>(pk[i >> 4], sel), a[i], oa);
    PIN(oa); SB();
    a[i] = ldnt(rv, voff, i, xw ^ T8); SB();
    if constexpr (i < 16) { wr[i] = __builtin_amdgcn_raw_buffer_load_b128(ro, (lane >> 3) * (DM * 2) + (lane & 7) * 16, (i ^ ox) * 8 * (DM * 2), 0); SB(); }
  });
  v4u mlo = __builtin_amdgcn_raw_buffer_load_b128(rg, WS_ML + (b * 64 + (j ^ 1)) * 16, 0, AUX_SC1);
  SB();
  LAS unsigned char* const wst = (LAS unsigned char*)lds + L_W + w * 4096;
  int aw, ar, yo;
  sfor<W>([&](auto I) {
    constexpr int i = I.v;
    oa = mfma(diagA<i & 15>(pk[2 + (i >> 4)], sel), a[i], oa);
    PIN(oa); SB();
    if constexpr (i == 24) {
      aw = L_A + (2 * w + (g >> 1)) * A_STR + 16 * c + 8 * (g & 1); ar = L_A + c * A_STR + 16 * g; yo = WS_Y + (b * 64 + j) * 4096 + tid * 8;
      PIN(aw); PIN(ar); PIN(yo); SB();
    }
    if constexpr (i >= 8 && i < 24 && (i & 3) == 0) {
      constexpr int s = (i - 8) >> 2;
      sfor<4>([&](auto U) { *(LAS v4u*)(wst + (U.v ^ (ox & 3)) * 1024 + lane * 16) = wr[4 * s + U.v]; });
      sfor<4>([&](auto T) {
        LAS unsigned char* p = wst + (8 * g + (c >> 2)) * 128 + 32 * T.v + 8 * (c & 3);
        const v2u lo = __builtin_bit_cast(v2u, __builtin_amdgcn_ds_read_tr16_b64_v4i16((LAS s16x4*)p));
        const v2u hi = __builtin_bit_cast(v2u, __builtin_amdgcn_ds_read_tr16_b64_v4i16((LAS s16x4*)(p + 512)));
        wr[4 * s + T.v] = v4u{lo.x, lo.y, hi.x, hi.y};
      });
      SB();
    }
  });
  *reinterpret_cast<v2u*>(lds + aw) = v2u{cvtpk(oa.x, oa.y), cvtpk(oa.z, oa.w)};
  SB();
  if (!__all(mlo.y == tag32 && mlo.w == tag32)) {
    const unsigned char* src = ws + WS_ML + (b * 64 + (j ^ 1)) * 16;
#pragma unroll 1
    for (unsigned spins = 0;; ++spins) {
      asm volatile("global_load_dwordx4 %0, %1, off sc1\n\ts_waitcnt vmcnt(0)" : "=&v"(mlo) : "v"(src) : "memory");
      if (__all(mlo.y == tag32 && mlo.w == tag32)) break;
      if (spins > SPIN_CAP) { mlo.x = mlo.z = 0x7fc00000u; break; }
      __builtin_amdgcn_s_sleep(2);
    }
  }
  __syncthreads();
  {
    v4u A[4];
    sfor<4>([&](auto S) { A[S.v] = *reinterpret_cast<const v4u*>(lds + ar + 64 * (S.v ^ (ox >> 2))); });
    SB();
    PIN(mlo.x);
    const float Mo = __uint_as_float(mlo.x), Lo = __uint_as_float(mlo.z);
    const float Mh = fmaxf(M, Mo), e0 = __builtin_amdgcn_exp2f((M - Mh) * C2), e1 = __builtin_amdgcn_exp2f((Mo - Mh) * C2);
    const float cw = e0 / (e0 * L + e1 * Lo);
    f32x4 y[4] = {};
    sfor<4>([&](auto S) { sfor<4>([&](auto T) { y[T.v] = mfma(A[S.v], wr[4 * S.v + T.v], y[T.v]); }); });
    float v[4];
    sfor<4>([&](auto T) { v[T.v] = (y[T.v].x + y[T.v].y) + (y[T.v].z + y[T.v].w); });
    const float yc = swap16_add(swap32_add(v[0], v[2]), swap32_add(v[1], v[3])) * cw;
    const unsigned yb = gran(yc, t8), yn = dppu<XOR1>(yb);
    if (!(lane & 1)) __builtin_amdgcn_raw_buffer_store_b128(v4u{yb, tag32, yn, tag32}, rg, yo, 0, AUX_SC1);
  }
  if (w == 0) {
    const unsigned char* src = ws + WS_Y + (b * 64 + (lane >> 2)) * 4096 + j * 64 + (lane & 3) * 16;
    const unsigned char *s1 = src + 65536, *s2 = src + 2 * 65536, *s3 = src + 3 * 65536;
    v4u g0, g1, g2, g3;
#pragma unroll 1
    for (unsigned spins = 0;; ++spins) {
      asm volatile("global_load_dwordx4 %0, %4, off sc1\n\tglobal_load_dwordx4 %1, %5, off sc1\n\tglobal_load_dwordx4 %2, %6, off sc1\n\tglobal_load_dwordx4 %3, %7, off sc1\n\t"
                   "s_waitcnt vmcnt(0)"
                   : "=&v"(g0), "=&v"(g1), "=&v"(g2), "=&v"(g3) : "v"(src), "v"(s1), "v"(s2), "v"(s3) : "memory");
      if (__all((gbad(g0, tag32, t8) | gbad(g1, tag32, t8) | gbad(g2, tag32, t8) | gbad(g3, tag32, t8)) == 0)) break;
      if (spins > SPIN_CAP) { g0.x = g0.z = 0x7fc00000u; break; }
    }
    float r0 = (gval(g0.x) + gval(g1.x)) + (gval(g2.x) + gval(g3.x)), r1 = (gval(g0.z) + gval(g1.z)) + (gval(g2.z) + gval(g3.z));
    r0 += dppf<ROR4>(r0); r1 += dppf<ROR4>(r1);
    r0 += dppf<XOR8>(r0); r1 += dppf<XOR8>(r1);
    float r = swap16_add(r0, r1);
    r = swap32_add(r, r);
    if (lane < 32 && c < 4) out[b * DM + 8 * j + 2 * c + g] = (uint16_t)(cvtpk(r, 0.f) & 0xffffu);
    if (lane == 0 && j == 0) __hip_atomic_store((gu64*)(ws + WS_EPOCH + b * 256), tag, __ATOMIC_RELAXED, __HIP_MEMORY_SCOPE_AGENT);
  }
}

extern "C" void kernel_launch(void* const* d_in, const int* in_sizes, int n_in, void* d_out, int out_size, void* d_ws, size_t ws_size, hipStream_t stream) {
  (void)in_sizes; (void)n_in; (void)out_size; (void)ws_size;
  mha_decode_fused<<<NWG, NTHR, LDS_BYTES, stream>>>((const uint16_t*)d_in[0], (const uint16_t*)d_in[1], (const uint16_t*)d_in[2], (const uint16_t*)d_in[3],
                                                     (const uint16_t*)d_in[4], (unsigned char*)d_ws, (uint16_t*)d_out);
}
```
